```python
import torch

def _softmax(x, axis=-1):
    return torch.softmax(x, dim=axis)

def _gelu(x):
    return torch.nn.functional.gelu(x, approximate='tanh')

def _silu(x):
    return torch.nn.functional.silu(x)

def _sigmoid(x):
    return torch.sigmoid(x)

def _layernorm(x, axis=-1, eps=1e-05):
    m = x.mean(dim=axis, keepdim=True)
    v = x.var(dim=axis, keepdim=True, unbiased=False)
    return (x - m) / torch.sqrt(v + eps)

def _scatter_add(tgt, idx, src, axis=0):
    return tgt.clone().index_add_(axis, idx, src)

def _segment_sum(src, seg, n):
    out = torch.zeros((n,) + src.shape[1:], dtype=src.dtype)
    return out.index_add_(0, seg, src)

def _pad(x, pw):
    flat = tuple((v for pair in reversed(pw) for v in pair))
    return torch.nn.functional.pad(x, flat)
kernel_name = 'concat_heads__memory_aug_recurrent_mem_tr__compressed_kv_cla__prompt'

def setup_inputs():

    _w = lambda a: (lambda t: t.to(torch.bfloat16) if t.is_floating_point() else t)(torch.as_tensor(a))

    def _A_setup():
        B, S, Dm = (1, 2048, 448)
        H, Dh = (16, 256)
        M, seg = (8, 32)
        seg = min(seg, S)
        S = S // seg * seg
        return {'x': _w((lambda *s: torch.randn(*s, dtype=torch.bfloat16) * 0.05)(B, S, Dm)), 'mem0': _w((lambda *s: torch.randn(*s, dtype=torch.bfloat16) * 0.4)(B, M, Dm)), 'Wq': _w((lambda *s: torch.randn(*s, dtype=torch.bfloat16) * 6.4)(Dm, H, Dh) * Dm ** (-0.5)), 'Wk': _w((lambda *s: torch.randn(*s, dtype=torch.bfloat16) * 6.4)(Dm, H, Dh) * Dm ** (-0.5)), 'Wv': _w((lambda *s: torch.randn(*s, dtype=torch.bfloat16) * 0.4)(Dm, H, Dh) * Dm ** (-0.5)), 'Wo': _w((lambda *s: torch.randn(*s, dtype=torch.bfloat16) * 0.4)(H, Dh, Dm) * (H * Dh) ** (-0.5)), 'seg_len': seg}
    _A_in = _A_setup()

    def _B_setup():
        B, H, Sq, Sk, D, M = (1, 16, 2048, 2048, 256, 448)
        Dc = 4 * D
        return {'x': _w((lambda *s: torch.randn(*s, dtype=torch.bfloat16) * 1.6)(B, Sq, M)), 'c_kv': _w((lambda *s: torch.randn(*s, dtype=torch.bfloat16) * 0.2)(B, Sk, Dc)), 'w_q': _w((lambda *s: torch.randn(*s, dtype=torch.bfloat16) * 3.2)(M, H, D) * M ** (-0.5)), 'w_uk': _w((lambda *s: torch.randn(*s, dtype=torch.bfloat16) * 3.2)(Dc, H, D) * Dc ** (-0.5)), 'w_uv': _w((lambda *s: torch.randn(*s, dtype=torch.bfloat16) * 0.2)(Dc, H, D) * Dc ** (-0.5)), 'w_o': _w((lambda *s: torch.randn(*s, dtype=torch.bfloat16) * 0.2)(H, D, M) * (H * D) ** (-0.5))}
    _B_in = _B_setup()
    return {'A_x': _A_in['x'], 'A_mem0': _A_in['mem0'], 'A_Wq': _A_in['Wq'], 'A_Wk': _A_in['Wk'], 'A_Wv': _A_in['Wv'], 'A_Wo': _A_in['Wo'], 'A_seg_len': _A_in['seg_len'], 'B_x': _B_in['x'], 'B_c_kv': _B_in['c_kv'], 'B_w_q': _B_in['w_q'], 'B_w_uk': _B_in['w_uk'], 'B_w_uv': _B_in['w_uv'], 'B_w_o': _B_in['w_o']}

def reference(A_x, A_mem0, A_Wq, A_Wk, A_Wv, A_Wo, A_seg_len, B_x, B_c_kv, B_w_q, B_w_uk, B_w_uv, B_w_o):

    def _A_ref(x, mem0, Wq, Wk, Wv, Wo, seg_len):
        B, S, Dm = x.shape
        H, Dh = (Wq.shape[1], Wq.shape[2])
        M = mem0.shape[1]
        scale = Dh ** (-0.5)
        n_seg = S // seg_len
        L = M + seg_len
        idx = torch.arange(L)
        mask = (idx[:, None] < M) | (idx[:, None] >= idx[None, :])
        mem = mem0
        outs = []
        for s in range(n_seg):
            xs = x[:, s * seg_len:(s + 1) * seg_len, :].clone()
            h = torch.concatenate([mem, xs], axis=1)
            q = torch.einsum('btd,dhe->bhte', h, Wq)
            k = torch.einsum('btd,dhe->bhte', h, Wk)
            v = torch.einsum('btd,dhe->bhte', h, Wv)
            scores = torch.einsum('bhqe,bhke->bhqk', q, k) * scale
            scores = torch.where(mask[None, None], scores, -1000000000.0)
            p = _softmax(scores, axis=-1)
            o = torch.einsum('bhqk,bhke->bhqe', p, v)
            o = torch.einsum('bhte,hed->btd', o, Wo)
            mem = o[:, :M, :].clone()
            outs.append(o[:, M:, :])
        return torch.concatenate(outs, axis=1)

    def _B_ref(x, c_kv, w_q, w_uk, w_uv, w_o):
        B, Sq, M = (x.shape[0], x.shape[1], x.shape[2])
        Sk = c_kv.shape[1]
        H, D = (w_q.shape[1], w_q.shape[2])
        q = torch.einsum('bqm,mhd->bhqd', x, w_q)
        k = torch.einsum('bkc,chd->bhkd', c_kv, w_uk)
        v = torch.einsum('bkc,chd->bhkd', c_kv, w_uv)
        scores = torch.einsum('bhqd,bhkd->bhqk', q, k) * D ** (-0.5)
        off = Sk - Sq
        qi = torch.arange(Sq)[:, None]
        kj = torch.arange(Sk)[None, :]
        cmask = kj <= qi + off
        scores = torch.where(cmask[None, None], scores, -1000000000.0)
        p = _softmax(scores, axis=-1)
        ctx = torch.einsum('bhqk,bhkd->bhqd', p, v)
        return torch.einsum('bhqd,hdm->bqm', ctx, w_o)
    a = _A_ref(A_x, A_mem0, A_Wq, A_Wk, A_Wv, A_Wo, A_seg_len)
    b = _B_ref(B_x, B_c_kv, B_w_q, B_w_uk, B_w_uv, B_w_o)
    return torch.concatenate([a, b], axis=-1)
```

```cpp
#include <hip/hip_runtime.h>
#include <hip/hip_bf16.h>
#include <stdint.h>

typedef unsigned short u16;
typedef unsigned int u32;
typedef unsigned long long u64;
typedef __bf16 bf8 __attribute__((ext_vector_type(8)));
typedef short s4 __attribute__((ext_vector_type(4)));
typedef short s8 __attribute__((ext_vector_type(8)));
typedef float f4 __attribute__((ext_vector_type(4)));
typedef float f16v __attribute__((ext_vector_type(16)));
typedef u32 u4v __attribute__((ext_vector_type(4)));
typedef u32 u2v __attribute__((ext_vector_type(2)));
#define LAS __attribute__((address_space(3)))
#define GAS __attribute__((address_space(1)))

static __device__ __forceinline__ u16 f2bf(float f) {
  u32 u = __float_as_uint(f);
  u += 0x7fffu + ((u >> 16) & 1u);
  return (u16)(u >> 16);
}
static __device__ __forceinline__ float bf2f(u16 x) { return __uint_as_float(((u32)x) << 16); }
static __device__ __forceinline__ u32 pack2(float a, float b) { return (u32)f2bf(a) | ((u32)f2bf(b) << 16); }
static __device__ __forceinline__ bf8 as_bf8(u4v v) { return __builtin_bit_cast(bf8, v); }
static __device__ __forceinline__ s4 tr_read(const char* lds_base, unsigned off) {
  return __builtin_amdgcn_ds_read_tr16_b64_v4i16((LAS s4*)(lds_base + off));
}

constexpr int S_ = 2048, DM = 448, NH = 16, DH = 256, HD = 4096, MEM = 8, SEG = 32, NSEG = 64, DC = 1024;

struct GemmArgs {
  const u16* A; const u16* B; u16* C;
  long sA, sB, sC;
  int lda, ldb, ldc;
  int M, N, K;
};
__device__ __forceinline__ unsigned off_b(unsigned row, unsigned ch) { return 256u * row + 16u * (ch ^ (((row & 3) << 2) | ((row >> 2) & 3))); }

template <int BN, bool B_KN, bool TRANS_OUT>
__global__ __launch_bounds__(256, 2) void gemm_kernel(GemmArgs g) {
  __shared__ __attribute__((aligned(16))) char lds[32768];
  char* ldsA = lds; char* ldsB = lds + 16384;
  const int tid = threadIdx.x, lane = tid & 63, wave = tid >> 6;
  const int wm = wave >> 1, wn = wave & 1;
  const int m0 = blockIdx.y * 128, n0 = blockIdx.x * BN;
  const u16* A = g.A + (long)blockIdx.z * g.sA;
  const u16* B = g.B + (long)blockIdx.z * g.sB;
  u16* C = g.C + (long)blockIdx.z * g.sC;
  constexpr int NT = BN / 32;
  constexpr int BCH = BN * 64 / 8 / 256;
  f4 acc[4][NT];
#pragma unroll
  for (int i = 0; i < 4; ++i)
#pragma unroll
    for (int j = 0; j < NT; ++j) acc[i][j] = f4{0.f, 0.f, 0.f, 0.f};
  u4v ra[4], rb[BCH];
  auto load_regs = [&](int k0) {
#pragma unroll
    for (int i = 0; i < 4; ++i) {
      int id = tid + 256 * i, row = id >> 3, ch = id & 7;
      int gr = m0 + row; gr = gr < g.M ? gr : g.M - 1;
      ra[i] = *(const u4v*)(A + (long)gr * g.lda + k0 + ch * 8);
    }
#pragma unroll
    for (int i = 0; i < BCH; ++i) {
      int id = tid + 256 * i;
      if (B_KN) {
        int row = id / (BN / 8), ch = id % (BN / 8);
        int gn = n0 + ch * 8; gn = gn < g.N ? gn : g.N - 8;
        rb[i] = *(const u4v*)(B + (long)(k0 + row) * g.ldb + gn);
      } else {
        int row = id >> 3, ch = id & 7;
        int gn = n0 + row; gn = gn < g.N ? gn : g.N - 1;
        rb[i] = *(const u4v*)(B + (long)gn * g.ldb + k0 + ch * 8);
      }
    }
  };
  auto store_lds = [&]() {
#pragma unroll
    for (int i = 0; i < 4; ++i) {
      int id = tid + 256 * i, row = id >> 3, ch = id & 7;
      *(u4v*)(ldsA + row * 128 + ((ch ^ (row & 7)) << 4)) = ra[i];
    }
#pragma unroll
    for (int i = 0; i < BCH; ++i) {
      int id = tid + 256 * i;
      if (B_KN) {
        int row = id / (BN / 8), ch = id % (BN / 8);
        *(u4v*)(ldsB + off_b(row, ch)) = rb[i];
      } else {
        int row = id >> 3, ch = id & 7;
        *(u4v*)(ldsB + row * 128 + ((ch ^ (row & 7)) << 4)) = rb[i];
      }
    }
  };
  const int nk = g.K / 64;
  load_regs(0);
  for (int kt = 0; kt < nk; ++kt) {
    __syncthreads();
    store_lds();
    __syncthreads();
    if (kt + 1 < nk) load_regs((kt + 1) * 64);
#pragma unroll
    for (int ks = 0; ks < 2; ++ks) {
      bf8 af[4], bfr[NT];
#pragma unroll
      for (int i = 0; i < 4; ++i) {
        int row = wm * 64 + i * 16 + (lane & 15), ch = ks * 4 + (lane >> 4);
        af[i] = *(const bf8*)(ldsA + row * 128 + ((ch ^ (row & 7)) << 4));
      }
#pragma unroll
      for (int j = 0; j < NT; ++j) {
        if (B_KN) {
          int c = wn * NT + j;
          unsigned gq = lane >> 4, q = (lane & 15) >> 2, p = lane & 3;
          s4 lo = tr_read(ldsB, off_b(32 * ks + 8 * gq + q, 2 * c + (p >> 1)) + 8 * (p & 1));
          s4 hi = tr_read(ldsB, off_b(32 * ks + 8 * gq + 4 + q, 2 * c + (p >> 1)) + 8 * (p & 1));
          s8 v = {lo[0], lo[1], lo[2], lo[3], hi[0], hi[1], hi[2], hi[3]};
          bfr[j] = __builtin_bit_cast(bf8, v);
        } else {
          int row = wn * (BN / 2) + j * 16 + (lane & 15), ch = ks * 4 + (lane >> 4);
          bfr[j] = *(const bf8*)(ldsB + row * 128 + ((ch ^ (row & 7)) << 4));
        }
      }
#pragma unroll
      for (int i = 0; i < 4; ++i)
#pragma unroll
        for (int j = 0; j < NT; ++j) {
          if (TRANS_OUT) acc[i][j] = __builtin_amdgcn_mfma_f32_16x16x32_bf16(af[i], bfr[j], acc[i][j], 0, 0, 0);
          else acc[i][j] = __builtin_amdgcn_mfma_f32_16x16x32_bf16(bfr[j], af[i], acc[i][j], 0, 0, 0);
        }
    }
  }
#pragma unroll
  for (int i = 0; i < 4; ++i)
#pragma unroll
    for (int j = 0; j < NT; ++j) {
      u2v v = {pack2(acc[i][j][0], acc[i][j][1]), pack2(acc[i][j][2], acc[i][j][3])};
      if (TRANS_OUT) {
        int m = m0 + wm * 64 + i * 16 + 4 * (lane >> 4), n = n0 + wn * (BN / 2) + j * 16 + (lane & 15);
        if (m < g.M && n < g.N) *(u2v*)(C + (long)n * g.ldc + m) = v;
      } else {
        int m = m0 + wm * 64 + i * 16 + (lane & 15), n = n0 + wn * (BN / 2) + j * 16 + 4 * (lane >> 4);
        if (m < g.M && n < g.N) *(u2v*)(C + (long)m * g.ldc + n) = v;
      }
    }
}
__global__ __launch_bounds__(256, 1) void attnB_kernel(const u16* __restrict__ Q, const u16* __restrict__ K,
                                                      const u16* __restrict__ V, u16* __restrict__ O) {
  extern __shared__ __attribute__((aligned(16))) char lds[];
  const int tid = threadIdx.x, lane = tid & 63, wave = tid >> 6;
  const int h = blockIdx.x & 15, qb = 15 - (blockIdx.x >> 4);
  const int q0 = qb * 128, qw = q0 + 32 * wave;
  const int hh = lane >> 5, l31 = lane & 31;
  const u16* Kh = K + h * 256; const u16* Vh = V + h * 256;
  bf8 qf[16];
  {
    const u16* qp = Q + (long)(qw + l31) * HD + h * 256 + 8 * hh;
#pragma unroll
    for (int ks = 0; ks < 16; ++ks) qf[ks] = *(const bf8*)(qp + 16 * ks);
  }
  auto issue = [&](int kt, int buf) {
    char* kb = lds + buf * 32768; char* vb = lds + 65536 + buf * 32768;
#pragma unroll
    for (int i = 0; i < 8; ++i) {
      int ins = wave * 8 + i;
      int row = 2 * ins + (lane >> 5), pos = lane & 31;
      const u16* ks = Kh + (long)(kt * 64 + row) * HD + ((pos ^ (row & 15)) << 3);
      const u16* vs = Vh + (long)(kt * 64 + row) * HD + ((pos ^ ((row & 3) << 2)) << 3);
      __builtin_amdgcn_global_load_lds((const GAS void*)ks, (LAS void*)(kb + ins * 1024), 16, 0, 0);
      __builtin_amdgcn_global_load_lds((const GAS void*)vs, (LAS void*)(vb + ins * 1024), 16, 0, 0);
    }
  };
  f16v o[8];
#pragma unroll
  for (int i = 0; i < 8; ++i)
#pragma unroll
    for (int j = 0; j < 16; ++j) o[i][j] = 0.f;
  float m = -INFINITY, lsum = 0.f;
  const float sc = 0.0625f * 1.4426950408889634f;
  const int nkt = (q0 + 128) / 64;
  issue(0, 0);
  for (int kt = 0; kt < nkt; ++kt) {
    asm volatile("s_waitcnt vmcnt(0)" ::: "memory");
    __syncthreads();
    if (kt + 1 < nkt) issue(kt + 1, (kt + 1) & 1);
    if (kt * 64 > qw + 31) continue;
    const char* kb = lds + (kt & 1) * 32768; const char* vb = lds + 65536 + (kt & 1) * 32768;
    f16v s[2];
#pragma unroll
    for (int st = 0; st < 2; ++st) {
#pragma unroll
      for (int j = 0; j < 16; ++j) s[st][j] = 0.f;
      int row = 32 * st + l31;
#pragma unroll
      for (int ks = 0; ks < 16; ++ks) {
        bf8 kf = *(const bf8*)(kb + row * 512 + (((2 * ks + hh) ^ (row & 15)) << 4));
        s[st] = __builtin_amdgcn_mfma_f32_32x32x16_bf16(kf, qf[ks], s[st], 0, 0, 0);
      }
    }
    const bool diag = (kt * 64 + 63 > qw);
    float mx = -INFINITY;
#pragma unroll
    for (int st = 0; st < 2; ++st)
#pragma unroll
      for (int j = 0; j < 16; ++j) {
        float v = s[st][j] * sc;
        if (diag) { int key = kt * 64 + 32 * st + (j & 3) + 8 * (j >> 2) + 4 * hh; if (key > qw + l31) v = -INFINITY; }
        s[st][j] = v; mx = fmaxf(mx, v);
      }
    mx = fmaxf(mx, __shfl_xor(mx, 32));
    float mn = fmaxf(m, mx);
    float alpha = exp2f(m - mn);
    m = mn;
    float ps = 0.f;
#pragma unroll
    for (int st = 0; st < 2; ++st)
#pragma unroll
      for (int j = 0; j < 16; ++j) { float p = exp2f(s[st][j] - mn); s[st][j] = p; ps += p; }
    lsum = lsum * alpha + ps;
#pragma unroll
    for (int i = 0; i < 8; ++i)
#pragma unroll
      for (int j = 0; j < 16; ++j) o[i][j] *= alpha;
    bf8 pf[2][2];
#pragma unroll
    for (int st = 0; st < 2; ++st)
#pragma unroll
      for (int ss = 0; ss < 2; ++ss) {
        u4v t = {pack2(s[st][8 * ss + 0], s[st][8 * ss + 1]), pack2(s[st][8 * ss + 2], s[st][8 * ss + 3]),
                 pack2(s[st][8 * ss + 4], s[st][8 * ss + 5]), pack2(s[st][8 * ss + 6], s[st][8 * ss + 7])};
        pf[st][ss] = as_bf8(t);
      }
    const int blk = (lane >> 4) & 1, q4 = (lane & 15) >> 2, p4 = lane & 3;
#pragma unroll
    for (int dt = 0; dt < 8; ++dt) {
#pragma unroll
      for (int st = 0; st < 2; ++st)
#pragma unroll
        for (int ss = 0; ss < 2; ++ss) {
          int r0 = 32 * st + 16 * ss + 4 * hh + q4, r1 = r0 + 8;
          int ch = 4 * dt + 2 * blk + (p4 >> 1);
          s4 lo = tr_read(vb, r0 * 512 + ((ch ^ ((r0 & 3) << 2)) << 4) + 8 * (p4 & 1));
          s4 hi = tr_read(vb, r1 * 512 + ((ch ^ ((r1 & 3) << 2)) << 4) + 8 * (p4 & 1));
          s8 v = {lo[0], lo[1], lo[2], lo[3], hi[0], hi[1], hi[2], hi[3]};
          o[dt] = __builtin_amdgcn_mfma_f32_32x32x16_bf16(__builtin_bit_cast(bf8, v), pf[st][ss], o[dt], 0, 0, 0);
        }
    }
  }
  lsum += __shfl_xor(lsum, 32);
  float inv = 1.f / lsum;
  u16* op = O + (long)(qw + l31) * HD + h * 256 + 4 * hh;
#pragma unroll
  for (int dt = 0; dt < 8; ++dt)
#pragma unroll
    for (int g = 0; g < 4; ++g) {
      u2v v = {pack2(o[dt][4 * g] * inv, o[dt][4 * g + 1] * inv), pack2(o[dt][4 * g + 2] * inv, o[dt][4 * g + 3] * inv)};
      *(u2v*)(op + 32 * dt + 8 * g) = v;
    }
}
struct ChainArgs {
  const u16* mem0; const u16* At; const u16* Bt; const u16* G; const u16* Y;
  u16* mems; u16* U; u64* gM; u64* gS; u64* gP; u32* err;
};
#define RLX __ATOMIC_RELAXED, __HIP_MEMORY_SCOPE_AGENT
constexpr int SPIN_CAP = 1 << 20;
__device__ __forceinline__ void put_gran(u64* p, u32 tag, u32 val) {
  __hip_atomic_store((GAS u64*)p, ((u64)tag << 32) | val, RLX);
}
template <int N>
__device__ __forceinline__ void get_gran(u64* const (&p)[N], u32 tag, u32 (&v)[N], u32* err) {
  for (int spins = 0;; ++spins) {
    bool ok = true;
#pragma unroll
    for (int k = 0; k < N; ++k) { u64 x = __hip_atomic_load((GAS u64*)p[k], RLX); v[k] = (u32)x; ok &= ((u32)(x >> 32) == tag); }
    if (ok) break;
    if (spins > SPIN_CAP) { __hip_atomic_store((GAS u32*)err, 1u, RLX); break; }
    if ((spins & 255) == 255 && __hip_atomic_load((GAS u32*)err, RLX)) break;
  }
}

constexpr int MROW = 912;
constexpr int ZROW = 240;
constexpr int PROW = 144;
constexpr int L_MEM = 0;
constexpr int L_Z = L_MEM + 16 * MROW;
constexpr int L_P = L_Z + 48 * ZROW;
constexpr int L_SB = L_P + 16 * PROW;
constexpr int L_SX = L_SB + 4 * 64 * 4;
constexpr int L_PB = L_SX + 8 * 32 * 4;
constexpr int L_END = L_PB + 896 * 4;

__device__ __forceinline__ void chain_body(const ChainArgs a, char* lds, int wg) {
  const int tid = threadIdx.x, lane = tid & 63, wave = tid >> 6;
  const int h = wg >> 2, jq = wg & 3;
  const int l15 = lane & 15, lg = lane >> 4;
  for (int i = tid; i < L_END / 4; i += 512) ((u32*)lds)[i] = 0;
  const bool isB = wave >= 4;
  const int w3 = wave & 3;
  const int ntile0 = 2 * w3;
  const bool two = w3 < 3;
  bf8 wf[2][14];
  {
    const u16* W = (isB ? a.Bt : a.At) + (long)h * DM * DM;
#pragma unroll
    for (int t = 0; t < 2; ++t)
#pragma unroll
      for (int ks = 0; ks < 14; ++ks) {
        int nt = two ? ntile0 + t : 6;
        wf[t][ks] = *(const bf8*)(W + (long)(112 * jq + 16 * nt + l15) * DM + 32 * ks + 8 * lg);
      }
  }
  s4 gf[7];
  u4v yv;
  auto prefetch = [&](int s) {
    if (w3 == 3) {
      const u16* gp = a.G + ((long)h * S_ + 32 * s + 16 * (isB ? 1 : 0) + l15) * DM + 112 * jq + 4 * lg;
#pragma unroll
      for (int ks = 0; ks < 7; ++ks) gf[ks] = *(const s4*)(gp + 16 * ks);
    }
    if (tid < 448) {
      int key = tid / 14, c8 = tid % 14;
      yv = *(const u4v*)(a.Y + ((long)h * S_ + 32 * s + key) * DM + 112 * jq + 8 * c8);
    }
  };
  prefetch(0);
  __syncthreads();
  for (int s = 0; s < NSEG; ++s) {
    const int par = s & 1;
    const u32 tag = s + 1;
    if (s == 0) {
      if (tid < 448) { int r = tid / 56, c8 = tid % 56; *(u4v*)(lds + L_MEM + r * MROW + c8 * 16) = *(const u4v*)(a.mem0 + r * DM + c8 * 8); }
    } else {
      u64* gm = a.gM + par * 1792;
      {
        u64* p[3] = {gm + tid, gm + tid + 512, gm + tid + 1024}; u32 v[3];
        get_gran<3>(p, tag, v, a.err);
#pragma unroll
        for (int k = 0; k < 3; ++k) { int idx = tid + 512 * k, rp = idx / 448, col = idx % 448;
          *(u16*)(lds + L_MEM + (2 * rp) * MROW + col * 2) = (u16)v[k]; *(u16*)(lds + L_MEM + (2 * rp + 1) * MROW + col * 2) = (u16)(v[k] >> 16); }
      }
      if (tid < 256) {
        u64* p[1] = {gm + tid + 1536}; u32 v[1];
        get_gran<1>(p, tag, v, a.err);
        int idx = tid + 1536, rp = idx / 448, col = idx % 448;
        *(u16*)(lds + L_MEM + (2 * rp) * MROW + col * 2) = (u16)v[0]; *(u16*)(lds + L_MEM + (2 * rp + 1) * MROW + col * 2) = (u16)(v[0] >> 16);
      }
    }
    __syncthreads();
    {
      f4 acc0 = {0.f, 0.f, 0.f, 0.f}, acc1 = {0.f, 0.f, 0.f, 0.f};
      const char* mrow = lds + L_MEM + l15 * MROW;
#pragma unroll
      for (int ks = 0; ks < 14; ++ks) {
        bf8 mf = *(const bf8*)(mrow + 64 * ks + 16 * lg);
        acc0 = __builtin_amdgcn_mfma_f32_16x16x32_bf16(wf[0][ks], mf, acc0, 0, 0, 0);
        acc1 = __builtin_amdgcn_mfma_f32_16x16x32_bf16(wf[1][ks], mf, acc1, 0, 0, 0);
      }
      if (!isB) {
        f4 sa = {0.f, 0.f, 0.f, 0.f};
        {
          u2v t0 = {pack2(acc0[0], acc0[1]), pack2(acc0[2], acc0[3])};
          s4 ma = *(const s4*)(mrow + (112 * jq + 16 * (two ? ntile0 : 6) + 4 * lg) * 2);
          sa = __builtin_amdgcn_mfma_f32_16x16x16bf16_1k(ma, __builtin_bit_cast(s4, t0), sa, 0, 0, 0);
          if (two) {
            u2v t1 = {pack2(acc1[0], acc1[1]), pack2(acc1[2], acc1[3])};
            s4 mb = *(const s4*)(mrow + (112 * jq + 16 * (ntile0 + 1) + 4 * lg) * 2);
            sa = __builtin_amdgcn_mfma_f32_16x16x16bf16_1k(mb, __builtin_bit_cast(s4, t1), sa, 0, 0, 0);
          }
        }
        if (lg < 2 && l15 < 8) {
          float* sb = (float*)(lds + L_SB) + w3 * 64 + l15 * 8 + 4 * lg;
          *(f4*)sb = sa;
        }
      } else {
        if (l15 < 8) {
          u2v t0 = {pack2(acc0[0], acc0[1]), pack2(acc0[2], acc0[3])};
          int c0 = 16 * (two ? ntile0 : 6) + 4 * lg;
          *(u2v*)(lds + L_Z + l15 * ZROW + c0 * 2) = t0;
          u16* up = a.U + (((long)s * NH + h) * 8 + l15) * DM + 112 * jq;
          *(u2v*)(up + c0) = t0;
          if (two) {
            u2v t1 = {pack2(acc1[0], acc1[1]), pack2(acc1[2], acc1[3])};
            *(u2v*)(lds + L_Z + l15 * ZROW + (c0 + 16) * 2) = t1;
            *(u2v*)(up + c0 + 16) = t1;
          }
        }
      }
      if (w3 == 3) {
        f4 sx = {0.f, 0.f, 0.f, 0.f};
#pragma unroll
        for (int ks = 0; ks < 7; ++ks) {
          s4 mb = *(const s4*)(mrow + (112 * jq + 16 * ks + 4 * lg) * 2);
          sx = __builtin_amdgcn_mfma_f32_16x16x16bf16_1k(gf[ks], mb, sx, 0, 0, 0);
        }
        if (l15 < 8) *(f4*)((float*)(lds + L_SX) + l15 * 32 + 16 * (isB ? 1 : 0) + 4 * lg) = sx;
      }
      if (tid < 448) { int key = tid / 14, c8 = tid % 14; *(u4v*)(lds + L_Z + (8 + key) * ZROW + c8 * 16) = yv; }
    }
    if (s == NSEG - 1) break;
    prefetch(s + 1);
    __syncthreads();
    if (wave == 0) {
      float v[5];
      const int i = lane >> 3;
#pragma unroll
      for (int e = 0; e < 5; ++e) {
        int k = (lane & 7) * 5 + e;
        const float* sb = (const float*)(lds + L_SB);
        v[e] = k < 8 ? (sb[i * 8 + k] + sb[64 + i * 8 + k]) + (sb[128 + i * 8 + k] + sb[192 + i * 8 + k])
                     : ((const float*)(lds + L_SX))[i * 32 + k - 8];
      }
      u64* gs = a.gS + (long)par * 64 * 320;
#pragma unroll
      for (int e = 0; e < 5; ++e) put_gran(gs + wg * 320 + lane * 5 + e, tag, __float_as_uint(v[e]));
      float part[4][5];
#pragma unroll
      for (int j = 0; j < 4; ++j) {
        if (j == jq) {
#pragma unroll
          for (int e = 0; e < 5; ++e) part[j][e] = v[e];
        } else {
          u64* b = gs + ((wg & ~3) + j) * 320 + lane * 5;
          u64* p[5] = {b, b + 1, b + 2, b + 3, b + 4}; u32 x[5];
          get_gran<5>(p, tag, x, a.err);
#pragma unroll
          for (int e = 0; e < 5; ++e) part[j][e] = __uint_as_float(x[e]);
        }
      }
      float sv[5], mx = -INFINITY;
#pragma unroll
      for (int e = 0; e < 5; ++e) { sv[e] = ((part[0][e] + part[1][e]) + (part[2][e] + part[3][e])) * 0.0625f; mx = fmaxf(mx, sv[e]); }
      mx = fmaxf(mx, __shfl_xor(mx, 1)); mx = fmaxf(mx, __shfl_xor(mx, 2)); mx = fmaxf(mx, __shfl_xor(mx, 4));
      float sum = 0.f;
#pragma unroll
      for (int e = 0; e < 5; ++e) { sv[e] = __expf(sv[e] - mx); sum += sv[e]; }
      sum += __shfl_xor(sum, 1); sum += __shfl_xor(sum, 2); sum += __shfl_xor(sum, 4);
      float inv = 1.f / sum;
#pragma unroll
      for (int e = 0; e < 5; ++e) *(u16*)(lds + L_P + i * PROW + ((lane & 7) * 5 + e) * 2) = f2bf(sv[e] * inv);
    }
    __syncthreads();
    if (wave < 7) {
      f4 oa = {0.f, 0.f, 0.f, 0.f};
      const int q4 = (lane & 15) >> 2, p4 = lane & 3;
#pragma unroll
      for (int ks = 0; ks < 3; ++ks) {
        s4 zf = tr_read(lds, L_Z + (16 * ks + 4 * lg + q4) * ZROW + (16 * wave + 4 * p4) * 2);
        s4 pf = *(const s4*)(lds + L_P + l15 * PROW + (16 * ks + 4 * lg) * 2);
        oa = __builtin_amdgcn_mfma_f32_16x16x16bf16_1k(zf, pf, oa, 0, 0, 0);
      }
      if (l15 < 8) {
        u64* gp = a.gP + (long)par * 64 * 896;
#pragma unroll
        for (int r = 0; r < 4; ++r) {
          int col = 112 * jq + 16 * wave + 4 * lg + r, cons = col / 7, cc = col % 7;
          put_gran(gp + cons * 896 + h * 56 + l15 * 7 + cc, tag, __float_as_uint(oa[r]));
        }
      }
    }
    if (tid < 448) {
      u64* gp = a.gP + (long)par * 64 * 896 + wg * 896 + tid * 2;
      u64* p[2] = {gp, gp + 1}; u32 x[2];
      get_gran<2>(p, tag, x, a.err);
      u2v t = {x[0], x[1]};
      *(u2v*)(lds + L_PB + tid * 8) = t;
    }
    __syncthreads();
    if (tid < 28) {
      int rp = tid / 7, cc = tid % 7;
      const float* pb = (const float*)(lds + L_PB);
      float a0 = 0.f, a1 = 0.f;
#pragma unroll
      for (int hh = 0; hh < 16; ++hh) { a0 += pb[hh * 56 + (2 * rp) * 7 + cc]; a1 += pb[hh * 56 + (2 * rp + 1) * 7 + cc]; }
      u32 pk = pack2(a0, a1);
      put_gran(a.gM + (par ^ 1) * 1792 + rp * 448 + 7 * wg + cc, tag + 1, pk);
      u16* mp = a.mems + (long)(s + 1) * 8 * DM + 7 * wg + cc;
      mp[(2 * rp) * DM] = (u16)pk; mp[(2 * rp + 1) * DM] = (u16)(pk >> 16);
    }
  }
}

__global__ __launch_bounds__(512, 2) void chain_kernel(ChainArgs a) {
  __shared__ __attribute__((aligned(16))) char lds[L_END];
  chain_body(a, lds, blockIdx.x);
}

__global__ __launch_bounds__(256, 2) void tailA_kernel(const u16* __restrict__ Qx, const u16* __restrict__ Kx,
                                                      const u16* __restrict__ Kmem, const u16* __restrict__ U,
                                                      const u16* __restrict__ Y, u16* __restrict__ out) {
  __shared__ __attribute__((aligned(16))) char lds[48 * 464 + 32 * 112 + 64];
  char* Z = lds;
  char* P = lds + 48 * 464;
  const int tid = threadIdx.x, lane = tid & 63, wave = tid >> 6;
  const int s = blockIdx.x >> 1, half = blockIdx.x & 1;
  const int l15 = lane & 15, lg = lane >> 4;
  for (int i = tid; i < (48 * 464 + 32 * 112) / 4; i += 256) ((u32*)lds)[i] = 0;
  f4 acc[7];
#pragma unroll
  for (int i = 0; i < 7; ++i) acc[i] = f4{0.f, 0.f, 0.f, 0.f};
  __syncthreads();
  for (int h = 0; h < NH; ++h) {
    for (int i = tid; i < 40 * 28; i += 256) {
      int r = i / 28, c8 = i % 28;
      const u16* src = r < 8 ? U + (((long)s * NH + h) * 8 + r) * DM : Y + ((long)h * S_ + 32 * s + r - 8) * DM;
      *(u4v*)(Z + r * 464 + c8 * 16) = *(const u4v*)(src + 224 * half + 8 * c8);
    }
    if (wave < 2) {
      f4 sc[3];
#pragma unroll
      for (int t = 0; t < 3; ++t) sc[t] = f4{0.f, 0.f, 0.f, 0.f};
      const u16* qp = Qx + (long)(32 * s + 16 * wave + l15) * HD + h * 256 + 8 * lg;
      const u16* kmp = Kmem + (long)(8 * s + (l15 & 7)) * HD + h * 256 + 8 * lg;
      const u16* kxp = Kx + (long)(32 * s + l15) * HD + h * 256 + 8 * lg;
#pragma unroll
      for (int ks = 0; ks < 8; ++ks) {
        bf8 qf = *(const bf8*)(qp + 32 * ks);
        bf8 k0 = *(const bf8*)(kmp + 32 * ks);
        bf8 k1 = *(const bf8*)(kxp + 32 * ks);
        bf8 k2 = *(const bf8*)(kxp + 16 * HD + 32 * ks);
        sc[0] = __builtin_amdgcn_mfma_f32_16x16x32_bf16(k0, qf, sc[0], 0, 0, 0);
        sc[1] = __builtin_amdgcn_mfma_f32_16x16x32_bf16(k1, qf, sc[1], 0, 0, 0);
        sc[2] = __builtin_amdgcn_mfma_f32_16x16x32_bf16(k2, qf, sc[2], 0, 0, 0);
      }
      const int q = 16 * wave + l15;
      float mx = -INFINITY;
#pragma unroll
      for (int t = 0; t < 3; ++t)
#pragma unroll
        for (int r = 0; r < 4; ++r) {
          int kk = 4 * lg + r;
          bool ok = t == 0 ? (kk < 8) : (16 * (t - 1) + kk <= q);
          float v = ok ? sc[t][r] * 0.0625f : -INFINITY;
          sc[t][r] = v; mx = fmaxf(mx, v);
        }
      mx = fmaxf(mx, __shfl_xor(mx, 16)); mx = fmaxf(mx, __shfl_xor(mx, 32));
      float sum = 0.f;
#pragma unroll
      for (int t = 0; t < 3; ++t)
#pragma unroll
        for (int r = 0; r < 4; ++r) { float p = __expf(sc[t][r] - mx); sc[t][r] = p; sum += p; }
      sum += __shfl_xor(sum, 16); sum += __shfl_xor(sum, 32);
      float inv = 1.f / sum;
      if (lg < 2) { u2v v = {pack2(sc[0][0] * inv, sc[0][1] * inv), pack2(sc[0][2] * inv, sc[0][3] * inv)}; *(u2v*)(P + q * 112 + (4 * lg) * 2) = v; }
#pragma unroll
      for (int t = 1; t < 3; ++t) {
        u2v v = {pack2(sc[t][0] * inv, sc[t][1] * inv), pack2(sc[t][2] * inv, sc[t][3] * inv)};
        *(u2v*)(P + q * 112 + (8 + 16 * (t - 1) + 4 * lg) * 2) = v;
      }
    }
    __syncthreads();
    {
      const int mt = wave & 1, nb = (wave >> 1) * 7;
      const int q4 = (lane & 15) >> 2, p4 = lane & 3;
#pragma unroll
      for (int ks = 0; ks < 3; ++ks) {
        s4 pf = *(const s4*)(P + (16 * mt + l15) * 112 + (16 * ks + 4 * lg) * 2);
#pragma unroll
        for (int j = 0; j < 7; ++j) {
          s4 zf = tr_read(Z, (16 * ks + 4 * lg + q4) * 464 + (16 * (nb + j) + 4 * p4) * 2);
          acc[j] = __builtin_amdgcn_mfma_f32_16x16x16bf16_1k(zf, pf, acc[j], 0, 0, 0);
        }
      }
    }
    __syncthreads();
  }
  {
    const int mt = wave & 1, nb = (wave >> 1) * 7;
    u16* op = out + (long)(32 * s + 16 * mt + l15) * 896 + 224 * half;
#pragma unroll
    for (int j = 0; j < 7; ++j) {
      u2v v = {pack2(acc[j][0], acc[j][1]), pack2(acc[j][2], acc[j][3])};
      *(u2v*)(op + 16 * (nb + j) + 4 * lg) = v;
    }
  }
}
static inline GemmArgs mk(const u16* A, const u16* B, u16* C, long sA, long sB, long sC, int lda, int ldb, int ldc, int M, int N, int K) {
  GemmArgs g; g.A = A; g.B = B; g.C = C; g.sA = sA; g.sB = sB; g.sC = sC; g.lda = lda; g.ldb = ldb; g.ldc = ldc; g.M = M; g.N = N; g.K = K; return g;
}
extern "C" void kernel_launch(void* const* d_in, const int* in_sizes, int n_in, void* d_out, int out_size, void* d_ws, size_t ws_size,
                              hipStream_t stream) {
  const u16* Ax = (const u16*)d_in[0]; const u16* mem0 = (const u16*)d_in[1];
  const u16* Wq = (const u16*)d_in[2]; const u16* Wk = (const u16*)d_in[3]; const u16* Wv = (const u16*)d_in[4]; const u16* Wo = (const u16*)d_in[5];
  const u16* Bx = (const u16*)d_in[7]; const u16* ckv = (const u16*)d_in[8];
  const u16* wq = (const u16*)d_in[9]; const u16* wuk = (const u16*)d_in[10]; const u16* wuv = (const u16*)d_in[11]; const u16* wo = (const u16*)d_in[12];
  u16* out = (u16*)d_out;
  char* ws = (char*)d_ws;
  size_t off = 0;
  auto alloc = [&](size_t bytes) { char* p = ws + off; off += (bytes + 255) & ~(size_t)255; return p; };
  u64* gM = (u64*)alloc(2 * 1792 * 8);
  u64* gS = (u64*)alloc(2 * 64 * 320 * 8);
  u64* gP = (u64*)alloc(2 * 64 * 896 * 8);
  u32* err = (u32*)alloc(256);
  size_t sync_bytes = off;
  const size_t big = (size_t)S_ * HD * 2;
  u16* Qx = (u16*)alloc(big); u16* Kx = (u16*)alloc(big); u16* Vx = (u16*)alloc(big);
  u16* At = (u16*)alloc((size_t)NH * DM * DM * 2); u16* Bt = (u16*)alloc((size_t)NH * DM * DM * 2);
  u16* G = (u16*)alloc((size_t)NH * S_ * DM * 2); u16* Y = (u16*)alloc((size_t)NH * S_ * DM * 2);
  u16* mems = (u16*)alloc((size_t)65 * 8 * DM * 2);
  u16* U = (u16*)alloc((size_t)NSEG * NH * 8 * DM * 2);
  u16* Kmem = (u16*)alloc((size_t)512 * HD * 2);
  u16* Qb = (u16*)alloc(big); u16* Kb = (u16*)alloc(big); u16* Vb = (u16*)alloc(big); u16* Ctx = (u16*)alloc(big);

  (void)hipMemsetAsync(ws, 0, sync_bytes, stream);
  (void)hipMemcpyAsync(mems, mem0, 8 * DM * 2, hipMemcpyDeviceToDevice, stream);
  gemm_kernel<128, true, false><<<dim3(32, 16, 1), 256, 0, stream>>>(mk(Ax, Wk, Kx, 0, 0, 0, DM, HD, HD, S_, HD, DM));
  gemm_kernel<128, true, false><<<dim3(32, 16, 1), 256, 0, stream>>>(mk(Ax, Wv, Vx, 0, 0, 0, DM, HD, HD, S_, HD, DM));
  gemm_kernel<128, true, false><<<dim3(32, 16, 1), 256, 0, stream>>>(mk(Ax, Wq, Qx, 0, 0, 0, DM, HD, HD, S_, HD, DM));
  gemm_kernel<64, false, false><<<dim3(7, 4, 16), 256, 0, stream>>>(mk(Wk, Wq, At, 256, 256, (long)DM * DM, HD, HD, DM, DM, DM, 256));
  gemm_kernel<64, true, true><<<dim3(7, 4, 16), 256, 0, stream>>>(mk(Wv, Wo, Bt, 256, (long)256 * DM, (long)DM * DM, HD, DM, DM, DM, DM, 256));
  gemm_kernel<64, false, false><<<dim3(7, 16, 16), 256, 0, stream>>>(mk(Kx, Wq, G, 256, 256, (long)S_ * DM, HD, HD, DM, S_, DM, 256));
  gemm_kernel<64, true, false><<<dim3(7, 16, 16), 256, 0, stream>>>(mk(Vx, Wo, Y, 256, (long)256 * DM, (long)S_ * DM, HD, DM, DM, S_, DM, 256));
  ChainArgs ca; ca.mem0 = mem0; ca.At = At; ca.Bt = Bt; ca.G = G; ca.Y = Y; ca.mems = mems; ca.U = U; ca.gM = gM; ca.gS = gS; ca.gP = gP; ca.err = err;
  chain_kernel<<<64, 512, 0, stream>>>(ca);
  gemm_kernel<128, true, false><<<dim3(32, 4, 1), 256, 0, stream>>>(mk(mems, Wk, Kmem, 0, 0, 0, DM, HD, HD, 512, HD, DM));
  tailA_kernel<<<128, 256, 0, stream>>>(Qx, Kx, Kmem, U, Y, out);
  gemm_kernel<128, true, false><<<dim3(32, 16, 1), 256, 0, stream>>>(mk(Bx, wq, Qb, 0, 0, 0, DM, HD, HD, S_, HD, DM));
  gemm_kernel<128, true, false><<<dim3(32, 16, 1), 256, 0, stream>>>(mk(ckv, wuk, Kb, 0, 0, 0, DC, HD, HD, S_, HD, DC));
  gemm_kernel<128, true, false><<<dim3(32, 16, 1), 256, 0, stream>>>(mk(ckv, wuv, Vb, 0, 0, 0, DC, HD, HD, S_, HD, DC));
  (void)hipFuncSetAttribute((const void*)attnB_kernel, hipFuncAttributeMaxDynamicSharedMemorySize, 131072);
  attnB_kernel<<<256, 256, 131072, stream>>>(Qb, Kb, Vb, Ctx);
  gemm_kernel<64, true, false><<<dim3(7, 16, 1), 256, 0, stream>>>(mk(Ctx, wo, out + 448, 0, 0, 0, HD, DM, 896, S_, DM, HD));
}
```

```cpp
#include <hip/hip_runtime.h>
#include <hip/hip_bf16.h>
#include <stdint.h>

typedef unsigned short u16;
typedef unsigned int u32;
typedef unsigned long long u64;
typedef __bf16 bf8 __attribute__((ext_vector_type(8)));
typedef short s4 __attribute__((ext_vector_type(4)));
typedef short s8 __attribute__((ext_vector_type(8)));
typedef float f4 __attribute__((ext_vector_type(4)));
typedef float f16v __attribute__((ext_vector_type(16)));
typedef u32 u4v __attribute__((ext_vector_type(4)));
typedef u32 u2v __attribute__((ext_vector_type(2)));
#define LAS __attribute__((address_space(3)))
#define GAS __attribute__((address_space(1)))

static __device__ __forceinline__ u16 f2bf(float f) {
  u32 u = __float_as_uint(f);
  u += 0x7fffu + ((u >> 16) & 1u);
  return (u16)(u >> 16);
}
static __device__ __forceinline__ float bf2f(u16 x) { return __uint_as_float(((u32)x) << 16); }
static __device__ __forceinline__ u32 pack2(float a, float b) { return (u32)f2bf(a) | ((u32)f2bf(b) << 16); }
static __device__ __forceinline__ bf8 as_bf8(u4v v) { return __builtin_bit_cast(bf8, v); }
static __device__ __forceinline__ s4 tr_read(const char* lds_base, unsigned off) {
  return __builtin_amdgcn_ds_read_tr16_b64_v4i16((LAS s4*)(lds_base + off));
}

constexpr int S_ = 2048, DM = 448, NH = 16, DH = 256, HD = 4096, MEM = 8, SEG = 32, NSEG = 64, DC = 1024;

struct GemmArgs {
  const u16* A; const u16* B; u16* C;
  long sA, sB, sC;
  int lda, ldb, ldc;
  int M, N, K;
};
__device__ __forceinline__ unsigned off_b(unsigned row, unsigned ch) { return 256u * row + 16u * (ch ^ (((row & 3) << 2) | ((row >> 2) & 3))); }

template <int BN, bool B_KN, bool TRANS_OUT>
__global__ __launch_bounds__(256, 2) void gemm_kernel(GemmArgs g) {
  __shared__ __attribute__((aligned(16))) char lds[32768];
  char* ldsA = lds; char* ldsB = lds + 16384;
  const int tid = threadIdx.x, lane = tid & 63, wave = tid >> 6;
  const int wm = wave >> 1, wn = wave & 1;
  const int m0 = blockIdx.y * 128, n0 = blockIdx.x * BN;
  const u16* A = g.A + (long)blockIdx.z * g.sA;
  const u16* B = g.B + (long)blockIdx.z * g.sB;
  u16* C = g.C + (long)blockIdx.z * g.sC;
  constexpr int NT = BN / 32;
  constexpr int BCH = BN * 64 / 8 / 256;
  f4 acc[4][NT];
#pragma unroll
  for (int i = 0; i < 4; ++i)
#pragma unroll
    for (int j = 0; j < NT; ++j) acc[i][j] = f4{0.f, 0.f, 0.f, 0.f};
  u4v ra[4], rb[BCH];
  auto load_regs = [&](int k0) {
#pragma unroll
    for (int i = 0; i < 4; ++i) {
      int id = tid + 256 * i, row = id >> 3, ch = id & 7;
      int gr = m0 + row; gr = gr < g.M ? gr : g.M - 1;
      ra[i] = *(const u4v*)(A + (long)gr * g.lda + k0 + ch * 8);
    }
#pragma unroll
    for (int i = 0; i < BCH; ++i) {
      int id = tid + 256 * i;
      if (B_KN) {
        int row = id / (BN / 8), ch = id % (BN / 8);
        int gn = n0 + ch * 8; gn = gn < g.N ? gn : g.N - 8;
        rb[i] = *(const u4v*)(B + (long)(k0 + row) * g.ldb + gn);
      } else {
        int row = id >> 3, ch = id & 7;
        int gn = n0 + row; gn = gn < g.N ? gn : g.N - 1;
        rb[i] = *(const u4v*)(B + (long)gn * g.ldb + k0 + ch * 8);
      }
    }
  };
  auto store_lds = [&]() {
#pragma unroll
    for (int i = 0; i < 4; ++i) {
      int id = tid + 256 * i, row = id >> 3, ch = id & 7;
      *(u4v*)(ldsA + row * 128 + ((ch ^ (row & 7)) << 4)) = ra[i];
    }
#pragma unroll
    for (int i = 0; i < BCH; ++i) {
      int id = tid + 256 * i;
      if (B_KN) {
        int row = id / (BN / 8), ch = id % (BN / 8);
        *(u4v*)(ldsB + off_b(row, ch)) = rb[i];
      } else {
        int row = id >> 3, ch = id & 7;
        *(u4v*)(ldsB + row * 128 + ((ch ^ (row & 7)) << 4)) = rb[i];
      }
    }
  };
  const int nk = g.K / 64;
  load_regs(0);
  for (int kt = 0; kt < nk; ++kt) {
    __syncthreads();
    store_lds();
    __syncthreads();
    if (kt + 1 < nk) load_regs((kt + 1) * 64);
#pragma unroll
    for (int ks = 0; ks < 2; ++ks) {
      bf8 af[4], bfr[NT];
#pragma unroll
      for (int i = 0; i < 4; ++i) {
        int row = wm * 64 + i * 16 + (lane & 15), ch = ks * 4 + (lane >> 4);
        af[i] = *(const bf8*)(ldsA + row * 128 + ((ch ^ (row & 7)) << 4));
      }
#pragma unroll
      for (int j = 0; j < NT; ++j) {
        if (B_KN) {
          int c = wn * NT + j;
          unsigned gq = lane >> 4, q = (lane & 15) >> 2, p = lane & 3;
          s4 lo = tr_read(ldsB, off_b(32 * ks + 8 * gq + q, 2 * c + (p >> 1)) + 8 * (p & 1));
          s4 hi = tr_read(ldsB, off_b(32 * ks + 8 * gq + 4 + q, 2 * c + (p >> 1)) + 8 * (p & 1));
          s8 v = {lo[0], lo[1], lo[2], lo[3], hi[0], hi[1], hi[2], hi[3]};
          bfr[j] = __builtin_bit_cast(bf8, v);
        } else {
          int row = wn * (BN / 2) + j * 16 + (lane & 15), ch = ks * 4 + (lane >> 4);
          bfr[j] = *(const bf8*)(ldsB + row * 128 + ((ch ^ (row & 7)) << 4));
        }
      }
#pragma unroll
      for (int i = 0; i < 4; ++i)
#pragma unroll
        for (int j = 0; j < NT; ++j) {
          if (TRANS_OUT) acc[i][j] = __builtin_amdgcn_mfma_f32_16x16x32_bf16(af[i], bfr[j], acc[i][j], 0, 0, 0);
          else acc[i][j] = __builtin_amdgcn_mfma_f32_16x16x32_bf16(bfr[j], af[i], acc[i][j], 0, 0, 0);
        }
    }
  }
#pragma unroll
  for (int i = 0; i < 4; ++i)
#pragma unroll
    for (int j = 0; j < NT; ++j) {
      u2v v = {pack2(acc[i][j][0], acc[i][j][1]), pack2(acc[i][j][2], acc[i][j][3])};
      if (TRANS_OUT) {
        int m = m0 + wm * 64 + i * 16 + 4 * (lane >> 4), n = n0 + wn * (BN / 2) + j * 16 + (lane & 15);
        if (m < g.M && n < g.N) *(u2v*)(C + (long)n * g.ldc + m) = v;
      } else {
        int m = m0 + wm * 64 + i * 16 + (lane & 15), n = n0 + wn * (BN / 2) + j * 16 + 4 * (lane >> 4);
        if (m < g.M && n < g.N) *(u2v*)(C + (long)m * g.ldc + n) = v;
      }
    }
}
__global__ __launch_bounds__(256, 1) void attnB_kernel(const u16* __restrict__ Q, const u16* __restrict__ K,
                                                      const u16* __restrict__ V, u16* __restrict__ O) {
  extern __shared__ __attribute__((aligned(16))) char lds[];
  const int tid = threadIdx.x, lane = tid & 63, wave = tid >> 6;
  const int h = blockIdx.x & 15, qb = 15 - (blockIdx.x >> 4);
  const int q0 = qb * 128, qw = q0 + 32 * wave;
  const int hh = lane >> 5, l31 = lane & 31;
  const u16* Kh = K + h * 256; const u16* Vh = V + h * 256;
  bf8 qf[16];
  {
    const u16* qp = Q + (long)(qw + l31) * HD + h * 256 + 8 * hh;
#pragma unroll
    for (int ks = 0; ks < 16; ++ks) qf[ks] = *(const bf8*)(qp + 16 * ks);
  }
  auto issue = [&](int kt, int buf) {
    char* kb = lds + buf * 32768; char* vb = lds + 65536 + buf * 32768;
#pragma unroll
    for (int i = 0; i < 8; ++i) {
      int ins = wave * 8 + i;
      int row = 2 * ins + (lane >> 5), pos = lane & 31;
      const u16* ks = Kh + (long)(kt * 64 + row) * HD + ((pos ^ (row & 15)) << 3);
      const u16* vs = Vh + (long)(kt * 64 + row) * HD + ((pos ^ ((row & 3) << 2)) << 3);
      __builtin_amdgcn_global_load_lds((const GAS void*)ks, (LAS void*)(kb + ins * 1024), 16, 0, 0);
      __builtin_amdgcn_global_load_lds((const GAS void*)vs, (LAS void*)(vb + ins * 1024), 16, 0, 0);
    }
  };
  f16v o[8];
#pragma unroll
  for (int i = 0; i < 8; ++i)
#pragma unroll
    for (int j = 0; j < 16; ++j) o[i][j] = 0.f;
  float m = -INFINITY, lsum = 0.f;
  const float sc = 0.0625f * 1.4426950408889634f;
  const int nkt = (q0 + 128) / 64;
  issue(0, 0);
  for (int kt = 0; kt < nkt; ++kt) {
    asm volatile("s_waitcnt vmcnt(0)" ::: "memory");
    __syncthreads();
    if (kt + 1 < nkt) issue(kt + 1, (kt + 1) & 1);
    if (kt * 64 > qw + 31) continue;
    const char* kb = lds + (kt & 1) * 32768; const char* vb = lds + 65536 + (kt & 1) * 32768;
    f16v s[2];
#pragma unroll
    for (int st = 0; st < 2; ++st) {
#pragma unroll
      for (int j = 0; j < 16; ++j) s[st][j] = 0.f;
      int row = 32 * st + l31;
#pragma unroll
      for (int ks = 0; ks < 16; ++ks) {
        bf8 kf = *(const bf8*)(kb + row * 512 + (((2 * ks + hh) ^ (row & 15)) << 4));
        s[st] = __builtin_amdgcn_mfma_f32_32x32x16_bf16(kf, qf[ks], s[st], 0, 0, 0);
      }
    }
    const bool diag = (kt * 64 + 63 > qw);
    float mx = -INFINITY;
#pragma unroll
    for (int st = 0; st < 2; ++st)
#pragma unroll
      for (int j = 0; j < 16; ++j) {
        float v = s[st][j] * sc;
        if (diag) { int key = kt * 64 + 32 * st + (j & 3) + 8 * (j >> 2) + 4 * hh; if (key > qw + l31) v = -INFINITY; }
        s[st][j] = v; mx = fmaxf(mx, v);
      }
    mx = fmaxf(mx, __shfl_xor(mx, 32));
    float mn = fmaxf(m, mx);
    float alpha = exp2f(m - mn);
    m = mn;
    float ps = 0.f;
#pragma unroll
    for (int st = 0; st < 2; ++st)
#pragma unroll
      for (int j = 0; j < 16; ++j) { float p = exp2f(s[st][j] - mn); s[st][j] = p; ps += p; }
    lsum = lsum * alpha + ps;
#pragma unroll
    for (int i = 0; i < 8; ++i)
#pragma unroll
      for (int j = 0; j < 16; ++j) o[i][j] *= alpha;
    bf8 pf[2][2];
#pragma unroll
    for (int st = 0; st < 2; ++st)
#pragma unroll
      for (int ss = 0; ss < 2; ++ss) {
        u4v t = {pack2(s[st][8 * ss + 0], s[st][8 * ss + 1]), pack2(s[st][8 * ss + 2], s[st][8 * ss + 3]),
                 pack2(s[st][8 * ss + 4], s[st][8 * ss + 5]), pack2(s[st][8 * ss + 6], s[st][8 * ss + 7])};
        pf[st][ss] = as_bf8(t);
      }
    const int blk = (lane >> 4) & 1, q4 = (lane & 15) >> 2, p4 = lane & 3;
#pragma unroll
    for (int dt = 0; dt < 8; ++dt) {
#pragma unroll
      for (int st = 0; st < 2; ++st)
#pragma unroll
        for (int ss = 0; ss < 2; ++ss) {
          int r0 = 32 * st + 16 * ss + 4 * hh + q4, r1 = r0 + 8;
          int ch = 4 * dt + 2 * blk + (p4 >> 1);
          s4 lo = tr_read(vb, r0 * 512 + ((ch ^ ((r0 & 3) << 2)) << 4) + 8 * (p4 & 1));
          s4 hi = tr_read(vb, r1 * 512 + ((ch ^ ((r1 & 3) << 2)) << 4) + 8 * (p4 & 1));
          s8 v = {lo[0], lo[1], lo[2], lo[3], hi[0], hi[1], hi[2], hi[3]};
          o[dt] = __builtin_amdgcn_mfma_f32_32x32x16_bf16(__builtin_bit_cast(bf8, v), pf[st][ss], o[dt], 0, 0, 0);
        }
    }
  }
  lsum += __shfl_xor(lsum, 32);
  float inv = 1.f / lsum;
  u16* op = O + (long)(qw + l31) * HD + h * 256 + 4 * hh;
#pragma unroll
  for (int dt = 0; dt < 8; ++dt)
#pragma unroll
    for (int g = 0; g < 4; ++g) {
      u2v v = {pack2(o[dt][4 * g] * inv, o[dt][4 * g + 1] * inv), pack2(o[dt][4 * g + 2] * inv, o[dt][4 * g + 3] * inv)};
      *(u2v*)(op + 32 * dt + 8 * g) = v;
    }
}
struct ChainArgs {
  const u16* x; const u16* mem0; const u16* At; const u16* Bs;
  u64* memG; u64* gP; u32* err;
};
#define RLX __ATOMIC_RELAXED, __HIP_MEMORY_SCOPE_AGENT
constexpr int SPIN_CAP = 1 << 20;
__device__ __forceinline__ void put_gran(u64* p, u32 tag, u32 val) {
  __hip_atomic_store((GAS u64*)p, ((u64)tag << 32) | val, RLX);
}
template <int N>
__device__ __forceinline__ void get_gran(u64* const (&p)[N], u32 tag, u32 (&v)[N], u32* err) {
  for (int spins = 0;; ++spins) {
    bool ok = true;
#pragma unroll
    for (int k = 0; k < N; ++k) { u64 x = __hip_atomic_load((GAS u64*)p[k], RLX); v[k] = (u32)x; ok &= ((u32)(x >> 32) == tag); }
    if (ok) break;
    if (spins > SPIN_CAP) { __hip_atomic_store((GAS u32*)err, 1u, RLX); break; }
    if ((spins & 255) == 255 && __hip_atomic_load((GAS u32*)err, RLX)) break;
  }
}
__device__ __forceinline__ int opq(int v) { asm volatile("" : "+v"(v)); return v; }
__device__ __forceinline__ s8 cat8(s4 a, s4 b) { s8 v = {a[0], a[1], a[2], a[3], b[0], b[1], b[2], b[3]}; return v; }
__device__ __forceinline__ s4 pk4(f4 a) { u2v t = {pack2(a[0], a[1]), pack2(a[2], a[3])}; return __builtin_bit_cast(s4, t); }
#define MFMA32(a, b, c) __builtin_amdgcn_mfma_f32_16x16x32_bf16(__builtin_bit_cast(bf8, a), __builtin_bit_cast(bf8, b), c, 0, 0, 0)
#define MFMA16(a, b, c) __builtin_amdgcn_mfma_f32_16x16x16bf16_1k(a, b, c, 0, 0, 0)
__device__ __forceinline__ unsigned hoff(unsigned row, unsigned c) {
  unsigned ch = c >> 3;
  return row * 896u + (((ch & ~7u) | ((ch & 7u) ^ (row & 7u))) << 4) + (c & 7u) * 2u;
}
constexpr int KB2[3] = {0, 16, 24};
constexpr int L_B = 0;
constexpr int L_H = 100352;
constexpr int L_S = L_H + 35840;
constexpr int L_O = L_S + 10240;
constexpr int L_END = L_O + 14336;
#define STAMP(k)

__device__ __forceinline__ void load_xseg(const u16* x, int seg, char* lds, int lane) {
  lane = opq(lane);
#pragma unroll
  for (int q = 0; q < 28; ++q) {
    int p = q * 64 + lane, row = p / 56, pos = p % 56;
    int ch = (pos & ~7) | ((pos & 7) ^ (row & 7));
    const u16* src = x + (long)(32 * seg + row) * DM + ch * 8;
    __builtin_amdgcn_global_load_lds((const GAS void*)src, (LAS void*)(lds + L_H + 7168 + q * 1024), 16, 0, 0);
  }
}

__device__ __forceinline__ void chain_body(const ChainArgs a, char* lds, int wg) {
  const int tid = threadIdx.x, lane = tid & 63, wave = __builtin_amdgcn_readfirstlane(tid >> 6);
  const int h = wg >> 2, jq = wg & 3;
  const int l15 = lane & 15, lg = lane >> 4;
  if (wave == 7) load_xseg(a.x, 0, lds, lane);
  bf8 wf[3][14], wh[7];
  {
    const u16* W = a.At + (long)h * DM * DM;
#pragma unroll
    for (int t = 0; t < 3; ++t)
#pragma unroll
      for (int ks = 0; ks < 14; ++ks) wf[t][ks] = *(const bf8*)(W + (long)(16 * (3 * wave + t) + l15) * DM + 32 * ks + 8 * lg);
#pragma unroll
    for (int i = 0; i < 7; ++i) wh[i] = *(const bf8*)(W + (long)(16 * (24 + (wave >> 1)) + l15) * DM + 32 * (7 * (wave & 1) + i) + 8 * lg);
  }
  for (int idx = wave; idx < 98; idx += 8) {
    int ks = idx / 7, nt = idx % 7;
    const u16* bp = a.Bs + (long)(112 * jq + 16 * nt + l15) * (NH * DM) + h * DM + 32 * ks + 4 * lg;
    u2v lo = *(const u2v*)(bp), hi = *(const u2v*)(bp + 16);
    u4v v = {lo[0], lo[1], hi[0], hi[1]};
    *(u4v*)(lds + L_B + idx * 1024 + lane * 16) = v;
  }
  if (tid < 448) { int r = tid / 56, c8 = tid % 56; *(u4v*)(lds + L_H + hoff(r, c8 * 8)) = *(const u4v*)(a.mem0 + r * DM + c8 * 8); }
  const unsigned mo0 = L_H + l15 * 896 + ((lg ^ (l15 & 7)) << 4);
  for (int s = 0; s < NSEG - 1; ++s) {
    const int par = s & 1;
    const u32 tag = s + 1;
    if (s > 0 && tid < 448) {
      const int tid = opq(threadIdx.x);
      u64* gm = a.memG + (long)s * 1792 + tid;
      u64* p[4] = {gm, gm + 448, gm + 896, gm + 1344}; u32 v[4];
      get_gran<4>(p, tag, v, a.err);
      int hi = tid >= 224 ? 1 : 0, cp = tid - 224 * hi;
#pragma unroll
      for (int k = 0; k < 4; ++k) *(u32*)(lds + L_H + hoff(2 * k + hi, 2 * cp)) = v[k];
    }
    STAMP(0);
    asm volatile("s_waitcnt vmcnt(0)" ::: "memory");
    __syncthreads();
    STAMP(1);
    {
      f4 acc[4];
#pragma unroll
      for (int t = 0; t < 4; ++t) acc[t] = f4{0.f, 0.f, 0.f, 0.f};
#pragma unroll
      for (int ks = 0; ks < 14; ++ks) {
        bf8 mf = *(const bf8*)(lds + (mo0 ^ ((ks & 1) << 6)) + (ks >> 1) * 128);
#pragma unroll
        for (int t = 0; t < 3; ++t) acc[t] = __builtin_amdgcn_mfma_f32_16x16x32_bf16(wf[t][ks], mf, acc[t], 0, 0, 0);
        if (ks < 7) {
          int k2 = 7 * (wave & 1) + ks;
          bf8 m2 = *(const bf8*)(lds + (mo0 ^ ((k2 & 1) << 6)) + (k2 >> 1) * 128);
          acc[3] = __builtin_amdgcn_mfma_f32_16x16x32_bf16(wh[ks], m2, acc[3], 0, 0, 0);
        }
      }
      s8 tb0 = cat8(pk4(acc[0]), pk4(acc[1])), tb1 = cat8(pk4(acc[2]), pk4(acc[3]));
      const int lane = opq(threadIdx.x) & 63, l15 = lane & 15, lg = lane >> 4;
      const int c0 = 16 * (3 * wave) + 4 * lg, c3 = 16 * (24 + (wave >> 1)) + 4 * lg;
#pragma unroll
      for (int kt = 0; kt < 3; ++kt) {
        int row = KB2[kt] + l15;
        s4 h0 = *(const s4*)(lds + L_H + hoff(row, c0)), h1 = *(const s4*)(lds + L_H + hoff(row, c0 + 16));
        s4 h2 = *(const s4*)(lds + L_H + hoff(row, c0 + 32)), h3 = *(const s4*)(lds + L_H + hoff(row, c3));
        f4 sa = {0.f, 0.f, 0.f, 0.f};
        sa = MFMA32(cat8(h0, h1), tb0, sa);
        sa = MFMA32(cat8(h2, h3), tb1, sa);
        if (l15 < 8 && (kt < 2 || lg >= 2)) {
          int gi = kt < 2 ? 4 * kt + lg : 6 + lg;
          *(f4*)(lds + L_S + ((wave * 10 + gi) * 8 + l15) * 16) = sa;
        }
      }
    }
    STAMP(2);
    __syncthreads();
    STAMP(3);
    {
      const int lane = opq(threadIdx.x) & 63, l15 = lane & 15, lg = lane >> 4;
      f4 sv[3];
#pragma unroll
      for (int kt = 0; kt < 3; ++kt) {
        int gi = kt < 2 ? 4 * kt + lg : 8 + (lg & 1);
        const char* sp = lds + L_S + (gi * 8 + (l15 & 7)) * 16;
        f4 v0 = *(const f4*)(sp) + *(const f4*)(sp + 1280), v1 = *(const f4*)(sp + 2560) + *(const f4*)(sp + 3840);
        f4 v2 = *(const f4*)(sp + 5120) + *(const f4*)(sp + 6400), v3 = *(const f4*)(sp + 7680) + *(const f4*)(sp + 8960);
        sv[kt] = ((v0 + v1) + (v2 + v3)) * 0.0625f;
        __builtin_amdgcn_sched_barrier(0);
      }
      if (lg < 2) sv[2] = f4{-INFINITY, -INFINITY, -INFINITY, -INFINITY};
      float mx = -INFINITY;
#pragma unroll
      for (int kt = 0; kt < 3; ++kt)
#pragma unroll
        for (int r = 0; r < 4; ++r) mx = fmaxf(mx, sv[kt][r]);
      mx = fmaxf(mx, __shfl_xor(mx, 16)); mx = fmaxf(mx, __shfl_xor(mx, 32));
      float sum = 0.f;
#pragma unroll
      for (int kt = 0; kt < 3; ++kt)
#pragma unroll
        for (int r = 0; r < 4; ++r) { float p = __expf(sv[kt][r] - mx); sv[kt][r] = p; sum += p; }
      sum += __shfl_xor(sum, 16); sum += __shfl_xor(sum, 32);
      float inv = 1.f / sum;
      s8 p01 = cat8(pk4(sv[0] * inv), pk4(sv[1] * inv)); s4 p2 = pk4(sv[2] * inv);
      const int kq = wave & 3, nh = wave >> 2;
      const int q4 = l15 >> 2, p4 = lane & 3;
      f4 o[4];
#pragma unroll
      for (int n = 0; n < 4; ++n) o[n] = f4{0.f, 0.f, 0.f, 0.f};
      const int ks0 = kq < 2 ? 4 * kq : 3 * kq + 2;
      const char* bp = lds + L_B + (ks0 * 7 + 4 * nh) * 1024 + lane * 16;
#pragma unroll
      for (int p = 0; p < 4; ++p) {
        if (p == 3 && kq >= 2) break;
        s4 rb[2];
#pragma unroll
        for (int u = 0; u < 2; ++u) {
          int cc = 32 * (ks0 + p) + 16 * u + 4 * p4;
          s4 t0 = tr_read(lds, L_H + hoff(4 * lg + q4, cc)), t1 = tr_read(lds, L_H + hoff(16 + 4 * lg + q4, cc));
          s4 t2 = tr_read(lds, L_H + hoff(24 + 4 * lg + q4, cc));
          const f4 z = {0.f, 0.f, 0.f, 0.f};
          f4 r = MFMA32(cat8(t0, t1), p01, z), r2 = MFMA16(t2, p2, z);
          rb[u] = pk4(r + r2);
        }
#pragma unroll
        for (int n = 0; n < 4; ++n) {
          if (n == 3 && nh == 1) break;
          o[n] = MFMA32(*(const s8*)(bp + (n + 7 * p) * 1024), cat8(rb[0], rb[1]), o[n]);
        }
        __builtin_amdgcn_sched_barrier(0);
      }
#pragma unroll
      for (int n = 0; n < 4; ++n) {
        if (n == 3 && nh == 1) break;
        if (l15 < 8) *(f4*)(lds + L_O + kq * 3584 + l15 * 448 + (16 * (4 * nh + n) + 4 * lg) * 4) = o[n];
      }
    }
    STAMP(4);
    __syncthreads();
    STAMP(5);
    if (wave == 7) { load_xseg(a.x, s + 1, lds, lane); }
    else {
      const int tid = opq(threadIdx.x), l15 = tid & 15;
      {
        int i = tid / 56, np = tid % 56;
        const char* op = lds + L_O + i * 448 + np * 8;
        typedef float f2 __attribute__((ext_vector_type(2)));
        f2 v = (*(const f2*)(op) + *(const f2*)(op + 3584)) + (*(const f2*)(op + 7168) + *(const f2*)(op + 10752));
        int cb = 2 * jq + (np >= 28 ? 1 : 0), vp = np >= 28 ? np - 28 : np;
        u64* gp = a.gP + (long)par * 64 * 896 + (i * 8 + cb) * 896 + (vp * 16 + h) * 2;
        put_gran(gp, tag, __float_as_uint(v[0])); put_gran(gp + 1, tag, __float_as_uint(v[1]));
      }
      STAMP(6);
      {
        u64* gp = a.gP + (long)par * 64 * 896 + wg * 896 + tid * 2;
        u64* p[2] = {gp, gp + 1}; u32 x[2];
        get_gran<2>(p, tag, x, a.err);
        float a0 = __uint_as_float(x[0]), a1 = __uint_as_float(x[1]);
#pragma unroll
        for (int m = 1; m < 16; m <<= 1) { a0 += __shfl_xor(a0, m); a1 += __shfl_xor(a1, m); }
        if (l15 == 0) put_gran(a.memG + (long)(s + 1) * 1792 + (wg >> 3) * 224 + (wg & 7) * 28 + (tid >> 4), tag + 1, pack2(a0, a1));
      }
      STAMP(7);
    }
  }
}

__global__ __launch_bounds__(512, 2) void chain_kernel(ChainArgs a) {
  extern __shared__ __attribute__((aligned(16))) char lds[];
  chain_body(a, lds, blockIdx.x);
}

constexpr int T_H = 0, T_S = 71680, T_END = 71680 + 81920;
__device__ __forceinline__ void tail1_item(const ChainArgs a, u16* __restrict__ Rp, int sp, int h, char* lds) {
  const int tid = threadIdx.x, lane = tid & 63, wave = tid >> 6;
  const int l15 = lane & 15, lg = lane >> 4;
  __syncthreads();
  if (wave < 7) {
#pragma unroll
    for (int qq = 0; qq < 8; ++qq) {
      int q = wave * 8 + qq, sg = q / 28, q2 = q % 28;
      int p = q2 * 64 + lane, row = p / 56, pos = p % 56;
      int ch = (pos & ~7) | ((pos & 7) ^ (row & 7));
      const u16* src = a.x + (long)(64 * sp + 32 * sg + row) * DM + ch * 8;
      __builtin_amdgcn_global_load_lds((const GAS void*)src, (LAS void*)(lds + T_H + sg * 35840 + 7168 + q2 * 1024), 16, 0, 0);
    }
  }
  if (tid < 448) {
#pragma unroll
    for (int sg = 0; sg < 2; ++sg) {
      int s = 2 * sp + sg;
      if (s == 0) { int r = tid / 56, c8 = tid % 56; *(u4v*)(lds + T_H + hoff(r, c8 * 8)) = *(const u4v*)(a.mem0 + r * DM + c8 * 8); }
      else {
        u64* gm = a.memG + (long)s * 1792 + tid;
        u64* p[4] = {gm, gm + 448, gm + 896, gm + 1344}; u32 v[4];
        get_gran<4>(p, s + 1, v, a.err);
        int hi = tid >= 224 ? 1 : 0, cp = tid - 224 * hi;
#pragma unroll
        for (int k = 0; k < 4; ++k) *(u32*)(lds + T_H + sg * 35840 + hoff(2 * k + hi, 2 * cp)) = v[k];
      }
    }
  }
  asm volatile("s_waitcnt vmcnt(0)" ::: "memory");
  __syncthreads();
  f4 acc[4][4];
#pragma unroll
  for (int t = 0; t < 4; ++t)
#pragma unroll
    for (int u = 0; u < 4; ++u) acc[t][u] = f4{0.f, 0.f, 0.f, 0.f};
  {
    const u16* W = a.At + (long)h * DM * DM + (long)(16 * 3 * wave + l15) * DM + 8 * lg;
    const u16* W3 = a.At + (long)h * DM * DM + (long)(16 * (24 + (wave >> 1)) + l15) * DM + 32 * 7 * (wave & 1) + 8 * lg;
#pragma unroll 2
    for (int ks = 0; ks < 14; ++ks) {
      bf8 w0 = *(const bf8*)(W + 32 * ks), w1 = *(const bf8*)(W + 16 * DM + 32 * ks), w2 = *(const bf8*)(W + 32 * DM + 32 * ks);
#pragma unroll
      for (int u = 0; u < 4; ++u) {
        bf8 xf = *(const bf8*)(lds + T_H + (u >> 1) * 35840 + hoff(8 + 16 * (u & 1) + l15, 32 * ks + 8 * lg));
        acc[0][u] = __builtin_amdgcn_mfma_f32_16x16x32_bf16(w0, xf, acc[0][u], 0, 0, 0);
        acc[1][u] = __builtin_amdgcn_mfma_f32_16x16x32_bf16(w1, xf, acc[1][u], 0, 0, 0);
        acc[2][u] = __builtin_amdgcn_mfma_f32_16x16x32_bf16(w2, xf, acc[2][u], 0, 0, 0);
      }
    }
#pragma unroll 1
    for (int i = 0; i < 7; ++i) {
      bf8 w3 = *(const bf8*)(W3 + 32 * i);
      int k2 = 7 * (wave & 1) + i;
#pragma unroll
      for (int u = 0; u < 4; ++u) {
        bf8 xf = *(const bf8*)(lds + T_H + (u >> 1) * 35840 + hoff(8 + 16 * (u & 1) + l15, 32 * k2 + 8 * lg));
        acc[3][u] = __builtin_amdgcn_mfma_f32_16x16x32_bf16(w3, xf, acc[3][u], 0, 0, 0);
      }
    }
  }
  {
    const int c0 = 16 * (3 * wave) + 4 * lg, c3 = 16 * (24 + (wave >> 1)) + 4 * lg;
#pragma unroll
    for (int u = 0; u < 4; ++u) {
      s8 tb0 = cat8(pk4(acc[0][u]), pk4(acc[1][u])), tb1 = cat8(pk4(acc[2][u]), pk4(acc[3][u]));
      const char* hb = lds + T_H + (u >> 1) * 35840;
#pragma unroll
      for (int kt = 0; kt < 3; ++kt) {
        int row = KB2[kt] + l15;
        s4 h0 = *(const s4*)(hb + hoff(row, c0)), h1 = *(const s4*)(hb + hoff(row, c0 + 16));
        s4 h2 = *(const s4*)(hb + hoff(row, c0 + 32)), h3 = *(const s4*)(hb + hoff(row, c3));
        f4 sa = {0.f, 0.f, 0.f, 0.f};
        sa = MFMA32(cat8(h0, h1), tb0, sa);
        sa = MFMA32(cat8(h2, h3), tb1, sa);
        if (kt < 2 || lg >= 2) {
          int gi = kt < 2 ? 4 * kt + lg : 6 + lg;
          *(f4*)(lds + T_S + (((wave * 4 + u) * 10 + gi) * 16 + l15) * 16) = sa;
        }
      }
    }
  }
  __syncthreads();
  {
    const int u = wave & 3, chalf = wave >> 2;
    const char* hb = lds + T_H + (u >> 1) * 35840;
    f4 sv[3];
    const int tq = 16 * (u & 1) + l15;
#pragma unroll
    for (int kt = 0; kt < 3; ++kt) {
      int gi = kt < 2 ? 4 * kt + lg : 8 + (lg & 1);
      const char* sp2 = lds + T_S + ((u * 10 + gi) * 16 + l15) * 16;
      f4 v0 = *(const f4*)(sp2) + *(const f4*)(sp2 + 10240), v1 = *(const f4*)(sp2 + 20480) + *(const f4*)(sp2 + 30720);
      f4 v2 = *(const f4*)(sp2 + 40960) + *(const f4*)(sp2 + 51200), v3 = *(const f4*)(sp2 + 61440) + *(const f4*)(sp2 + 71680);
      f4 v = ((v0 + v1) + (v2 + v3)) * 0.0625f;
#pragma unroll
      for (int r = 0; r < 4; ++r) {
        int kk = KB2[kt] + 4 * lg + r;
        bool ok = (kt < 2 || lg >= 2) && (kk - 8 <= tq);
        sv[kt][r] = ok ? v[r] : -INFINITY;
      }
    }
    float mx = -INFINITY;
#pragma unroll
    for (int kt = 0; kt < 3; ++kt)
#pragma unroll
      for (int r = 0; r < 4; ++r) mx = fmaxf(mx, sv[kt][r]);
    mx = fmaxf(mx, __shfl_xor(mx, 16)); mx = fmaxf(mx, __shfl_xor(mx, 32));
    float sum = 0.f;
#pragma unroll
    for (int kt = 0; kt < 3; ++kt)
#pragma unroll
      for (int r = 0; r < 4; ++r) { float p = __expf(sv[kt][r] - mx); sv[kt][r] = p; sum += p; }
    sum += __shfl_xor(sum, 16); sum += __shfl_xor(sum, 32);
    float inv = 1.f / sum;
    s8 p01 = cat8(pk4(sv[0] * inv), pk4(sv[1] * inv)); s4 p2 = pk4(sv[2] * inv);
    const int q4 = l15 >> 2, p4 = lane & 3;
    u16* rp = Rp + (long)(64 * sp + 16 * u + l15) * (NH * DM) + h * DM + 4 * lg;
#pragma unroll
    for (int v = 0; v < 14; ++v) {
      int ct = 14 * chalf + v, cc = 16 * ct + 4 * p4;
      s4 t0 = tr_read(hb, hoff(4 * lg + q4, cc)), t1 = tr_read(hb, hoff(16 + 4 * lg + q4, cc));
      s4 t2 = tr_read(hb, hoff(24 + 4 * lg + q4, cc));
      const f4 z = {0.f, 0.f, 0.f, 0.f};
      f4 r = MFMA32(cat8(t0, t1), p01, z) + MFMA16(t2, p2, z);
      u2v o = {pack2(r[0], r[1]), pack2(r[2], r[3])};
      *(u2v*)(rp + 16 * ct) = o;
    }
  }
}
__global__ __launch_bounds__(512, 2) void tail1_kernel(ChainArgs a, u16* Rp) {
  extern __shared__ __attribute__((aligned(16))) char lds[];
  tail1_item(a, Rp, blockIdx.x >> 4, blockIdx.x & 15, lds);
}
static inline GemmArgs mk(const u16* A, const u16* B, u16* C, long sA, long sB, long sC, int lda, int ldb, int ldc, int M, int N, int K) {
  GemmArgs g; g.A = A; g.B = B; g.C = C; g.sA = sA; g.sB = sB; g.sC = sC; g.lda = lda; g.ldb = ldb; g.ldc = ldc; g.M = M; g.N = N; g.K = K; return g;
}
extern "C" void kernel_launch(void* const* d_in, const int* in_sizes, int n_in, void* d_out, int out_size, void* d_ws, size_t ws_size,
                              hipStream_t stream) {
  const u16* Ax = (const u16*)d_in[0]; const u16* mem0 = (const u16*)d_in[1];
  const u16* Wq = (const u16*)d_in[2]; const u16* Wk = (const u16*)d_in[3]; const u16* Wv = (const u16*)d_in[4]; const u16* Wo = (const u16*)d_in[5];
  const u16* Bx = (const u16*)d_in[7]; const u16* ckv = (const u16*)d_in[8];
  const u16* wq = (const u16*)d_in[9]; const u16* wuk = (const u16*)d_in[10]; const u16* wuv = (const u16*)d_in[11]; const u16* wo = (const u16*)d_in[12];
  u16* out = (u16*)d_out;
  char* ws = (char*)d_ws;
  size_t off = 0;
  auto alloc = [&](size_t bytes) { char* p = ws + off; off += (bytes + 255) & ~(size_t)255; return p; };
  u64* memG = (u64*)alloc(64 * 1792 * 8);
  u64* gP = (u64*)alloc(2 * 64 * 896 * 8);
  u32* err = (u32*)alloc(4096 + 64 * 384 * 8);
  size_t sync_bytes = off;
  const size_t big = (size_t)S_ * HD * 2;
  u16* At = (u16*)alloc((size_t)NH * DM * DM * 2); u16* Bs = (u16*)alloc((size_t)NH * DM * DM * 2);
  u16* Rp = (u16*)alloc((size_t)S_ * NH * DM * 2);
  u16* Qb = (u16*)alloc(big); u16* Kb = (u16*)alloc(big); u16* Vb = (u16*)alloc(big); u16* Ctx = (u16*)alloc(big);

  (void)hipMemsetAsync(ws, 0, sync_bytes, stream);
  gemm_kernel<64, false, false><<<dim3(7, 4, 16), 256, 0, stream>>>(mk(Wk, Wq, At, 256, 256, (long)DM * DM, HD, HD, DM, DM, DM, 256));
  gemm_kernel<64, true, true><<<dim3(7, 4, 16), 256, 0, stream>>>(mk(Wv, Wo, Bs, 256, (long)256 * DM, DM, HD, DM, NH * DM, DM, DM, 256));
  ChainArgs ca; ca.x = Ax; ca.mem0 = mem0; ca.At = At; ca.Bs = Bs; ca.memG = memG; ca.gP = gP; ca.err = err;
  (void)hipFuncSetAttribute((const void*)chain_kernel, hipFuncAttributeMaxDynamicSharedMemorySize, L_END);
  chain_kernel<<<64, 512, L_END, stream>>>(ca);
  (void)hipFuncSetAttribute((const void*)tail1_kernel, hipFuncAttributeMaxDynamicSharedMemorySize, T_END);
  tail1_kernel<<<512, 512, T_END, stream>>>(ca, Rp);
  gemm_kernel<64, false, false><<<dim3(7, 16, 1), 256, 0, stream>>>(mk(Rp, Bs, out, 0, 0, 0, NH * DM, NH * DM, 896, S_, DM, NH * DM));
  gemm_kernel<128, true, false><<<dim3(32, 16, 1), 256, 0, stream>>>(mk(Bx, wq, Qb, 0, 0, 0, DM, HD, HD, S_, HD, DM));
  gemm_kernel<128, true, false><<<dim3(32, 16, 1), 256, 0, stream>>>(mk(ckv, wuk, Kb, 0, 0, 0, DC, HD, HD, S_, HD, DC));
  gemm_kernel<128, true, false><<<dim3(32, 16, 1), 256, 0, stream>>>(mk(ckv, wuv, Vb, 0, 0, 0, DC, HD, HD, S_, HD, DC));
  (void)hipFuncSetAttribute((const void*)attnB_kernel, hipFuncAttributeMaxDynamicSharedMemorySize, 131072);
  attnB_kernel<<<256, 256, 131072, stream>>>(Qb, Kb, Vb, Ctx);
  gemm_kernel<64, true, false><<<dim3(7, 16, 1), 256, 0, stream>>>(mk(Ctx, wo, out + 448, 0, 0, 0, HD, DM, 896, S_, DM, HD));
}
```

```cpp
#include <hip/hip_runtime.h>
#include <hip/hip_bf16.h>
#include <stdint.h>

typedef unsigned short u16;
typedef unsigned int u32;
typedef unsigned long long u64;
typedef __bf16 bf8 __attribute__((ext_vector_type(8)));
typedef short s4 __attribute__((ext_vector_type(4)));
typedef short s8 __attribute__((ext_vector_type(8)));
typedef float f4 __attribute__((ext_vector_type(4)));
typedef float f16v __attribute__((ext_vector_type(16)));
typedef u32 u4v __attribute__((ext_vector_type(4)));
typedef u32 u2v __attribute__((ext_vector_type(2)));
#define LAS __attribute__((address_space(3)))
#define GAS __attribute__((address_space(1)))

static __device__ __forceinline__ u16 f2bf(float f) {
  u32 u = __float_as_uint(f);
  u += 0x7fffu + ((u >> 16) & 1u);
  return (u16)(u >> 16);
}
static __device__ __forceinline__ float bf2f(u16 x) { return __uint_as_float(((u32)x) << 16); }
static __device__ __forceinline__ u32 pack2(float a, float b) { return (u32)f2bf(a) | ((u32)f2bf(b) << 16); }
static __device__ __forceinline__ bf8 as_bf8(u4v v) { return __builtin_bit_cast(bf8, v); }
static __device__ __forceinline__ s4 tr_read(const char* lds_base, unsigned off) {
  return __builtin_amdgcn_ds_read_tr16_b64_v4i16((LAS s4*)(lds_base + off));
}

constexpr int S_ = 2048, DM = 448, NH = 16, DH = 256, HD = 4096, MEM = 8, SEG = 32, NSEG = 64, DC = 1024;

struct GemmArgs {
  const u16* A; const u16* B; u16* C;
  long sA, sB, sC;
  int lda, ldb, ldc;
  int M, N, K;
};
__device__ __forceinline__ unsigned off_b(unsigned row, unsigned ch) { return 256u * row + 16u * (ch ^ (((row & 3) << 2) | ((row >> 2) & 3))); }

template <int BN, bool B_KN, bool TRANS_OUT>
__global__ __launch_bounds__(256, 2) void gemm_kernel(GemmArgs g) {
  __shared__ __attribute__((aligned(16))) char lds[32768];
  char* ldsA = lds; char* ldsB = lds + 16384;
  const int tid = threadIdx.x, lane = tid & 63, wave = tid >> 6;
  const int wm = wave >> 1, wn = wave & 1;
  const int m0 = blockIdx.y * 128, n0 = blockIdx.x * BN;
  const u16* A = g.A + (long)blockIdx.z * g.sA;
  const u16* B = g.B + (long)blockIdx.z * g.sB;
  u16* C = g.C + (long)blockIdx.z * g.sC;
  constexpr int NT = BN / 32;
  constexpr int BCH = BN * 64 / 8 / 256;
  f4 acc[4][NT];
#pragma unroll
  for (int i = 0; i < 4; ++i)
#pragma unroll
    for (int j = 0; j < NT; ++j) acc[i][j] = f4{0.f, 0.f, 0.f, 0.f};
  u4v ra[4], rb[BCH];
  auto load_regs = [&](int k0) {
#pragma unroll
    for (int i = 0; i < 4; ++i) {
      int id = tid + 256 * i, row = id >> 3, ch = id & 7;
      int gr = m0 + row; gr = gr < g.M ? gr : g.M - 1;
      ra[i] = *(const u4v*)(A + (long)gr * g.lda + k0 + ch * 8);
    }
#pragma unroll
    for (int i = 0; i < BCH; ++i) {
      int id = tid + 256 * i;
      if (B_KN) {
        int row = id / (BN / 8), ch = id % (BN / 8);
        int gn = n0 + ch * 8; gn = gn < g.N ? gn : g.N - 8;
        rb[i] = *(const u4v*)(B + (long)(k0 + row) * g.ldb + gn);
      } else {
        int row = id >> 3, ch = id & 7;
        int gn = n0 + row; gn = gn < g.N ? gn : g.N - 1;
        rb[i] = *(const u4v*)(B + (long)gn * g.ldb + k0 + ch * 8);
      }
    }
  };
  auto store_lds = [&]() {
#pragma unroll
    for (int i = 0; i < 4; ++i) {
      int id = tid + 256 * i, row = id >> 3, ch = id & 7;
      *(u4v*)(ldsA + row * 128 + ((ch ^ (row & 7)) << 4)) = ra[i];
    }
#pragma unroll
    for (int i = 0; i < BCH; ++i) {
      int id = tid + 256 * i;
      if (B_KN) {
        int row = id / (BN / 8), ch = id % (BN / 8);
        *(u4v*)(ldsB + off_b(row, ch)) = rb[i];
      } else {
        int row = id >> 3, ch = id & 7;
        *(u4v*)(ldsB + row * 128 + ((ch ^ (row & 7)) << 4)) = rb[i];
      }
    }
  };
  const int nk = g.K / 64;
  load_regs(0);
  for (int kt = 0; kt < nk; ++kt) {
    __syncthreads();
    store_lds();
    __syncthreads();
    if (kt + 1 < nk) load_regs((kt + 1) * 64);
#pragma unroll
    for (int ks = 0; ks < 2; ++ks) {
      bf8 af[4], bfr[NT];
#pragma unroll
      for (int i = 0; i < 4; ++i) {
        int row = wm * 64 + i * 16 + (lane & 15), ch = ks * 4 + (lane >> 4);
        af[i] = *(const bf8*)(ldsA + row * 128 + ((ch ^ (row & 7)) << 4));
      }
#pragma unroll
      for (int j = 0; j < NT; ++j) {
        if (B_KN) {
          int c = wn * NT + j;
          unsigned gq = lane >> 4, q = (lane & 15) >> 2, p = lane & 3;
          s4 lo = tr_read(ldsB, off_b(32 * ks + 8 * gq + q, 2 * c + (p >> 1)) + 8 * (p & 1));
          s4 hi = tr_read(ldsB, off_b(32 * ks + 8 * gq + 4 + q, 2 * c + (p >> 1)) + 8 * (p & 1));
          s8 v = {lo[0], lo[1], lo[2], lo[3], hi[0], hi[1], hi[2], hi[3]};
          bfr[j] = __builtin_bit_cast(bf8, v);
        } else {
          int row = wn * (BN / 2) + j * 16 + (lane & 15), ch = ks * 4 + (lane >> 4);
          bfr[j] = *(const bf8*)(ldsB + row * 128 + ((ch ^ (row & 7)) << 4));
        }
      }
#pragma unroll
      for (int i = 0; i < 4; ++i)
#pragma unroll
        for (int j = 0; j < NT; ++j) {
          if (TRANS_OUT) acc[i][j] = __builtin_amdgcn_mfma_f32_16x16x32_bf16(af[i], bfr[j], acc[i][j], 0, 0, 0);
          else acc[i][j] = __builtin_amdgcn_mfma_f32_16x16x32_bf16(bfr[j], af[i], acc[i][j], 0, 0, 0);
        }
    }
  }
#pragma unroll
  for (int i = 0; i < 4; ++i)
#pragma unroll
    for (int j = 0; j < NT; ++j) {
      u2v v = {pack2(acc[i][j][0], acc[i][j][1]), pack2(acc[i][j][2], acc[i][j][3])};
      if (TRANS_OUT) {
        int m = m0 + wm * 64 + i * 16 + 4 * (lane >> 4), n = n0 + wn * (BN / 2) + j * 16 + (lane & 15);
        if (m < g.M && n < g.N) *(u2v*)(C + (long)n * g.ldc + m) = v;
      } else {
        int m = m0 + wm * 64 + i * 16 + (lane & 15), n = n0 + wn * (BN / 2) + j * 16 + 4 * (lane >> 4);
        if (m < g.M && n < g.N) *(u2v*)(C + (long)m * g.ldc + n) = v;
      }
    }
}
__global__ __launch_bounds__(256, 1) void attnB_kernel(const u16* __restrict__ Q, const u16* __restrict__ K,
                                                      const u16* __restrict__ V, u16* __restrict__ O) {
  extern __shared__ __attribute__((aligned(16))) char lds[];
  const int tid = threadIdx.x, lane = tid & 63, wave = tid >> 6;
  const int h = blockIdx.x & 15, qb = 15 - (blockIdx.x >> 4);
  const int q0 = qb * 128, qw = q0 + 32 * wave;
  const int hh = lane >> 5, l31 = lane & 31;
  const u16* Kh = K + h * 256; const u16* Vh = V + h * 256;
  bf8 qf[16];
  {
    const u16* qp = Q + (long)(qw + l31) * HD + h * 256 + 8 * hh;
#pragma unroll
    for (int ks = 0; ks < 16; ++ks) qf[ks] = *(const bf8*)(qp + 16 * ks);
  }
  auto issue = [&](int kt, int buf) {
    char* kb = lds + buf * 32768; char* vb = lds + 65536 + buf * 32768;
#pragma unroll
    for (int i = 0; i < 8; ++i) {
      int ins = wave * 8 + i;
      int row = 2 * ins + (lane >> 5), pos = lane & 31;
      const u16* ks = Kh + (long)(kt * 64 + row) * HD + ((pos ^ (row & 15)) << 3);
      const u16* vs = Vh + (long)(kt * 64 + row) * HD + ((pos ^ ((row & 3) << 2)) << 3);
      __builtin_amdgcn_global_load_lds((const GAS void*)ks, (LAS void*)(kb + ins * 1024), 16, 0, 0);
      __builtin_amdgcn_global_load_lds((const GAS void*)vs, (LAS void*)(vb + ins * 1024), 16, 0, 0);
    }
  };
  f16v o[8];
#pragma unroll
  for (int i = 0; i < 8; ++i)
#pragma unroll
    for (int j = 0; j < 16; ++j) o[i][j] = 0.f;
  float m = -INFINITY, lsum = 0.f;
  const float sc = 0.0625f * 1.4426950408889634f;
  const int nkt = (q0 + 128) / 64;
  issue(0, 0);
  for (int kt = 0; kt < nkt; ++kt) {
    asm volatile("s_waitcnt vmcnt(0)" ::: "memory");
    __syncthreads();
    if (kt + 1 < nkt) issue(kt + 1, (kt + 1) & 1);
    if (kt * 64 > qw + 31) continue;
    const char* kb = lds + (kt & 1) * 32768; const char* vb = lds + 65536 + (kt & 1) * 32768;
    f16v s[2];
#pragma unroll
    for (int st = 0; st < 2; ++st) {
#pragma unroll
      for (int j = 0; j < 16; ++j) s[st][j] = 0.f;
      int row = 32 * st + l31;
#pragma unroll
      for (int ks = 0; ks < 16; ++ks) {
        bf8 kf = *(const bf8*)(kb + row * 512 + (((2 * ks + hh) ^ (row & 15)) << 4));
        s[st] = __builtin_amdgcn_mfma_f32_32x32x16_bf16(kf, qf[ks], s[st], 0, 0, 0);
      }
    }
    const bool diag = (kt * 64 + 63 > qw);
    float mx = -INFINITY;
#pragma unroll
    for (int st = 0; st < 2; ++st)
#pragma unroll
      for (int j = 0; j < 16; ++j) {
        float v = s[st][j] * sc;
        if (diag) { int key = kt * 64 + 32 * st + (j & 3) + 8 * (j >> 2) + 4 * hh; if (key > qw + l31) v = -INFINITY; }
        s[st][j] = v; mx = fmaxf(mx, v);
      }
    mx = fmaxf(mx, __shfl_xor(mx, 32));
    float mn = fmaxf(m, mx);
    float alpha = exp2f(m - mn);
    m = mn;
    float ps = 0.f;
#pragma unroll
    for (int st = 0; st < 2; ++st)
#pragma unroll
      for (int j = 0; j < 16; ++j) { float p = exp2f(s[st][j] - mn); s[st][j] = p; ps += p; }
    lsum = lsum * alpha + ps;
#pragma unroll
    for (int i = 0; i < 8; ++i)
#pragma unroll
      for (int j = 0; j < 16; ++j) o[i][j] *= alpha;
    bf8 pf[2][2];
#pragma unroll
    for (int st = 0; st < 2; ++st)
#pragma unroll
      for (int ss = 0; ss < 2; ++ss) {
        u4v t = {pack2(s[st][8 * ss + 0], s[st][8 * ss + 1]), pack2(s[st][8 * ss + 2], s[st][8 * ss + 3]),
                 pack2(s[st][8 * ss + 4], s[st][8 * ss + 5]), pack2(s[st][8 * ss + 6], s[st][8 * ss + 7])};
        pf[st][ss] = as_bf8(t);
      }
    const int blk = (lane >> 4) & 1, q4 = (lane & 15) >> 2, p4 = lane & 3;
#pragma unroll
    for (int dt = 0; dt < 8; ++dt) {
#pragma unroll
      for (int st = 0; st < 2; ++st)
#pragma unroll
        for (int ss = 0; ss < 2; ++ss) {
          int r0 = 32 * st + 16 * ss + 4 * hh + q4, r1 = r0 + 8;
          int ch = 4 * dt + 2 * blk + (p4 >> 1);
          s4 lo = tr_read(vb, r0 * 512 + ((ch ^ ((r0 & 3) << 2)) << 4) + 8 * (p4 & 1));
          s4 hi = tr_read(vb, r1 * 512 + ((ch ^ ((r1 & 3) << 2)) << 4) + 8 * (p4 & 1));
          s8 v = {lo[0], lo[1], lo[2], lo[3], hi[0], hi[1], hi[2], hi[3]};
          o[dt] = __builtin_amdgcn_mfma_f32_32x32x16_bf16(__builtin_bit_cast(bf8, v), pf[st][ss], o[dt], 0, 0, 0);
        }
    }
  }
  lsum += __shfl_xor(lsum, 32);
  float inv = 1.f / lsum;
  u16* op = O + (long)(qw + l31) * HD + h * 256 + 4 * hh;
#pragma unroll
  for (int dt = 0; dt < 8; ++dt)
#pragma unroll
    for (int g = 0; g < 4; ++g) {
      u2v v = {pack2(o[dt][4 * g] * inv, o[dt][4 * g + 1] * inv), pack2(o[dt][4 * g + 2] * inv, o[dt][4 * g + 3] * inv)};
      *(u2v*)(op + 32 * dt + 8 * g) = v;
    }
}
struct ChainArgs {
  const u16* x; const u16* mem0; const u16* At; const u16* Bs;
  u64* sy;
};
constexpr int SY_MEMG = 0, SY_GP = 64 * 1792, SY_MEMR = SY_GP + 2 * 64 * 512, SY_ERR = SY_MEMR + 2 * 8 * 1792, SY_END = SY_ERR + 512 + 64 * 384;
#define RLX __ATOMIC_RELAXED, __HIP_MEMORY_SCOPE_AGENT
constexpr int SPIN_CAP = 1 << 20;
__device__ __forceinline__ void put_gran(u64* p, u32 tag, u32 val) {
  __hip_atomic_store((GAS u64*)p, ((u64)tag << 32) | val, RLX);
}
template <int N>
__device__ __forceinline__ void get_gran(u64* const (&p)[N], u32 tag, u32 (&v)[N], u32* err) {
  for (int spins = 0;; ++spins) {
    bool ok = true;
#pragma unroll
    for (int k = 0; k < N; ++k) { u64 x = __hip_atomic_load((GAS u64*)p[k], RLX); v[k] = (u32)x; ok &= ((u32)(x >> 32) == tag); }
    if (ok) break;
    if (spins > SPIN_CAP) { __hip_atomic_store((GAS u32*)err, 1u, RLX); break; }
    if ((spins & 255) == 255 && __hip_atomic_load((GAS u32*)err, RLX)) break;
  }
}
__device__ __forceinline__ int opq(int v) { asm volatile("" : "+v"(v)); return v; }
__device__ __forceinline__ s8 cat8(s4 a, s4 b) { s8 v = {a[0], a[1], a[2], a[3], b[0], b[1], b[2], b[3]}; return v; }
__device__ __forceinline__ s4 pk4(f4 a) { u2v t = {pack2(a[0], a[1]), pack2(a[2], a[3])}; return __builtin_bit_cast(s4, t); }
#define MFMA32(a, b, c) __builtin_amdgcn_mfma_f32_16x16x32_bf16(__builtin_bit_cast(bf8, a), __builtin_bit_cast(bf8, b), c, 0, 0, 0)
#define MFMA16(a, b, c) __builtin_amdgcn_mfma_f32_16x16x16bf16_1k(a, b, c, 0, 0, 0)
__device__ __forceinline__ unsigned hoff(unsigned row, unsigned c) {
  unsigned ch = c >> 3;
  return row * 896u + (((ch & ~7u) | ((ch & 7u) ^ (row & 7u))) << 4) + (c & 7u) * 2u;
}
constexpr int KB2[3] = {0, 16, 24};
constexpr int KB3[3] = {0, 8, 24};
constexpr int L_B = 0;
constexpr int L_H = 100352;
constexpr int L_S = L_H + 35840;
constexpr int L_S2 = L_S;
constexpr int L_W = L_S + 10240;
constexpr int L_END = L_W + 16384;
#define STAMP(k)

__device__ __forceinline__ void load_xseg(const u16* x, int seg, char* lds, int lane) {
  lane = opq(lane);
  int lb = L_H + 7168; asm volatile("" : "+s"(lb));
  const char* xb = (const char*)(x + (long)32 * seg * DM);
  if (lane < 56) {
#pragma unroll
    for (int row = 0; row < 32; ++row) {
      const char* src = (xb + row * 896) + (unsigned)((lane ^ (row & 7)) << 4);
      __builtin_amdgcn_global_load_lds((const GAS void*)src, (LAS void*)(lds + lb + row * 896), 16, 0, 0);
    }
  }
}

typedef __bf16 bf2 __attribute__((ext_vector_type(2)));
__device__ __forceinline__ u32 cvt2(float a, float b) { bf2 r = {(__bf16)a, (__bf16)b}; return __builtin_bit_cast(u32, r); }
__device__ __forceinline__ s4 ck4(f4 a) { u2v t = {cvt2(a[0], a[1]), cvt2(a[2], a[3])}; return __builtin_bit_cast(s4, t); }
template <int CTRL> __device__ __forceinline__ float dppf(float v) {
  return __int_as_float(__builtin_amdgcn_update_dpp(0, __float_as_int(v), CTRL, 0xf, 0xf, true));
}
__device__ __forceinline__ float swap16_max(float v) { auto r = __builtin_amdgcn_permlane16_swap(__float_as_uint(v), __float_as_uint(v), false, false); return fmaxf(__uint_as_float(r[0]), __uint_as_float(r[1])); }
__device__ __forceinline__ float swap32_max(float v) { auto r = __builtin_amdgcn_permlane32_swap(__float_as_uint(v), __float_as_uint(v), false, false); return fmaxf(__uint_as_float(r[0]), __uint_as_float(r[1])); }
__device__ __forceinline__ float swap16_add(float v) { auto r = __builtin_amdgcn_permlane16_swap(__float_as_uint(v), __float_as_uint(v), false, false); return __uint_as_float(r[0]) + __uint_as_float(r[1]); }
__device__ __forceinline__ float swap32_add(float v) { auto r = __builtin_amdgcn_permlane32_swap(__float_as_uint(v), __float_as_uint(v), false, false); return __uint_as_float(r[0]) + __uint_as_float(r[1]); }

__device__ __forceinline__ s4 vx_tile(const char* lds, int wave, int r0) {
  const int lane = opq(threadIdx.x) & 63, l15 = lane & 15, lg = lane >> 4;
  const unsigned r1 = L_H + (r0 + l15) * 896 + ((lg ^ (l15 & 7)) << 4), r2 = r1 ^ 64;
  const char* bp = lds + L_B + wave * 1024 + lane * 16;
  f4 a1 = {0.f, 0.f, 0.f, 0.f}, a2 = {0.f, 0.f, 0.f, 0.f};
#pragma unroll
  for (int ks = 0; ks < 14; ks += 2) {
    a1 = __builtin_amdgcn_mfma_f32_16x16x32_bf16(*(const bf8*)(lds + r1 + (ks >> 1) * 128), *(const bf8*)(bp + ks * 7168), a1, 0, 0, 0);
    a2 = __builtin_amdgcn_mfma_f32_16x16x32_bf16(*(const bf8*)(lds + r2 + (ks >> 1) * 128), *(const bf8*)(bp + (ks + 1) * 7168), a2, 0, 0, 0);
  }
  return ck4(a1 + a2);
}

__device__ __forceinline__ void chain_body(const ChainArgs a, char* lds, int wg) {
  const int tid = threadIdx.x, lane = tid & 63, wave = __builtin_amdgcn_readfirstlane(tid >> 6);
  const int h = wg >> 2, jq = wg & 3;
  const int l15 = lane & 15, lg = lane >> 4;
  if (wave == 7) load_xseg(a.x, 0, lds, lane);
  bf8 wf[3][14], wh[5];
  {
    const u16* W = a.At + (long)h * DM * DM;
#pragma unroll
    for (int t = 0; t < 3; ++t)
#pragma unroll
      for (int ks = 0; ks < 14; ++ks) wf[t][ks] = *(const bf8*)(W + (long)(16 * (3 * wave + t) + l15) * DM + 32 * ks + 8 * lg);
#pragma unroll
    for (int i = 0; i < 7; ++i) {
      bf8 t = *(const bf8*)(W + (long)(16 * (24 + (wave >> 1)) + l15) * DM + 32 * (7 * (wave & 1) + i) + 8 * lg);
      if (i < 5) wh[i] = t; else *(bf8*)(lds + L_W + (wave * 2 + i - 5) * 1024 + lane * 16) = t;
    }
  }
  for (int idx = wave; idx < 98; idx += 8) {
    int ks = idx / 7, nt = idx % 7;
    *(u4v*)(lds + L_B + idx * 1024 + lane * 16) = *(const u4v*)(a.Bs + (long)(112 * jq + 16 * nt + l15) * (NH * DM) + h * DM + 32 * ks + 8 * lg);
  }
  if (tid < 448) { int r = tid / 56, c8 = tid % 56; *(u4v*)(lds + L_H + hoff(r, c8 * 8)) = *(const u4v*)(a.mem0 + r * DM + c8 * 8); }
  asm volatile("s_waitcnt vmcnt(0)" ::: "memory");
  __syncthreads();
  s4 vx1 = {0, 0, 0, 0}, vx2 = {0, 0, 0, 0};
  if (wave < 7) { vx1 = vx_tile(lds, wave, 8); vx2 = vx_tile(lds, wave, 24); }
  for (int s = 0; s < NSEG - 1; ++s) {
    const int par = s & 1;
    const u32 tag = s + 1;
    if (s > 0 && tid < 448) {
      const int tid = opq(threadIdx.x);
      u64* gm = (a.sy + SY_MEMR) + (long)(par * 8 + (wg & 7)) * 1792 + tid;
      u64* p[4] = {gm, gm + 448, gm + 896, gm + 1344}; u32 v[4];
      get_gran<4>(p, tag, v, (u32*)(a.sy + SY_ERR));
      int hi = tid >= 224 ? 1 : 0, cp = tid - 224 * hi;
#pragma unroll
      for (int k = 0; k < 4; ++k) *(u32*)(lds + L_H + hoff(2 * k + hi, 2 * cp)) = v[k];
    }
    STAMP(0);
    __syncthreads();
    STAMP(1);
    {
      f4 acc[4];
#pragma unroll
      for (int t = 0; t < 4; ++t) acc[t] = f4{0.f, 0.f, 0.f, 0.f};
      const int lane0 = opq(threadIdx.x) & 63;
      const unsigned mo0 = L_H + (lane0 & 15) * 896 + (((lane0 >> 4) ^ (lane0 & 7)) << 4), mo1 = mo0 ^ 64;
      const unsigned e0 = (wave & 1) ? mo1 + 384 : mo0, e1 = (wave & 1) ? mo0 + 512 : mo1;
#pragma unroll
      for (int ks = 0; ks < 14; ++ks) {
        bf8 mf = *(const bf8*)(lds + ((ks & 1) ? mo1 : mo0) + (ks >> 1) * 128);
#pragma unroll
        for (int t = 0; t < 3; ++t) acc[t] = __builtin_amdgcn_mfma_f32_16x16x32_bf16(wf[t][ks], mf, acc[t], 0, 0, 0);
        if (ks < 7) {
          bf8 m2 = *(const bf8*)(lds + ((ks & 1) ? e1 : e0) + (ks >> 1) * 128);
          acc[3] = __builtin_amdgcn_mfma_f32_16x16x32_bf16(ks < 5 ? wh[ks < 5 ? ks : 0] : *(const bf8*)(lds + L_W + (wave * 2 + ks - 5) * 1024 + lane0 * 16), m2, acc[3], 0, 0, 0);
        }
      }
      s8 tb0 = cat8(ck4(acc[0]), ck4(acc[1])), tb1 = cat8(ck4(acc[2]), ck4(acc[3]));
      const int lane = opq(threadIdx.x) & 63, l15 = lane & 15, lg = lane >> 4;
      const int c0 = 16 * (3 * wave) + 4 * lg, c3 = 16 * (24 + (wave >> 1)) + 4 * lg;
#pragma unroll
      for (int kt = 0; kt < 3; ++kt) {
        int row = KB3[kt] + l15;
        s4 h0 = *(const s4*)(lds + L_H + hoff(row, c0)), h1 = *(const s4*)(lds + L_H + hoff(row, c0 + 16));
        s4 h2 = *(const s4*)(lds + L_H + hoff(row, c0 + 32)), h3 = *(const s4*)(lds + L_H + hoff(row, c3));
        f4 sa = {0.f, 0.f, 0.f, 0.f};
        sa = MFMA32(cat8(h0, h1), tb0, sa);
        sa = MFMA32(cat8(h2, h3), tb1, sa);
        if (l15 < 8 && (kt > 0 || lg < 2)) {
          int gi = kt == 0 ? lg : 4 * kt - 2 + lg;
          *(f4*)(lds + L_S + ((wave * 10 + gi) * 8 + l15) * 16) = sa;
        }
      }
    }
    STAMP(2);
    __syncthreads();
    STAMP(3);
    if (tid < 320) {
      const int tid = opq(threadIdx.x);
      const float* sp = (const float*)(lds + L_S) + tid;
      float v = ((sp[0] + sp[320]) + (sp[640] + sp[960])) + ((sp[1280] + sp[1600]) + (sp[1920] + sp[2240]));
      ((float*)(lds + L_S2))[tid] = v * 0.0625f;
    }
    asm volatile("s_waitcnt lgkmcnt(0)\n\ts_barrier" ::: "memory");
    STAMP(8);
    if (wave < 7) {
      {
        const int lane = opq(threadIdx.x) & 63, l15 = lane & 15, lg = lane >> 4;
        f4 sv[3];
#pragma unroll
        for (int kt = 0; kt < 3; ++kt) {
          int gi = kt == 0 ? (lg & 1) : 4 * kt - 2 + lg;
          sv[kt] = *(const f4*)(lds + L_S2 + (gi * 8 + (l15 & 7)) * 16);
        }
        f4 va0 = {0.f, 0.f, 0.f, 0.f}, va1 = {0.f, 0.f, 0.f, 0.f};
        const unsigned mo0 = L_H + l15 * 896 + ((lg ^ (l15 & 7)) << 4), mo1 = mo0 ^ 64;
        const char* bp = lds + L_B + wave * 1024 + lane * 16;
        auto vgrp = [&](int k0, int k1) {
#pragma unroll
          for (int ks = k0; ks < k1; ks += 2) {
            va0 = __builtin_amdgcn_mfma_f32_16x16x32_bf16(*(const bf8*)(lds + mo0 + (ks >> 1) * 128), *(const bf8*)(bp + ks * 7168), va0, 0, 0, 0);
            va1 = __builtin_amdgcn_mfma_f32_16x16x32_bf16(*(const bf8*)(lds + mo1 + (ks >> 1) * 128), *(const bf8*)(bp + (ks + 1) * 7168), va1, 0, 0, 0);
          }
        };
        vgrp(0, 2);
        if (lg >= 2) sv[0] = f4{-INFINITY, -INFINITY, -INFINITY, -INFINITY};
        float mx = -INFINITY;
#pragma unroll
        for (int kt = 0; kt < 3; ++kt)
#pragma unroll
          for (int r = 0; r < 4; ++r) mx = fmaxf(mx, sv[kt][r]);
        mx = swap32_max(swap16_max(mx));
        __builtin_amdgcn_sched_barrier(0);
        vgrp(2, 4);
        float sum = 0.f;
#pragma unroll
        for (int kt = 0; kt < 3; ++kt)
#pragma unroll
          for (int r = 0; r < 4; ++r) { float p = __expf(sv[kt][r] - mx); sv[kt][r] = p; sum += p; }
        __builtin_amdgcn_sched_barrier(0);
        vgrp(4, 6);
        sum = swap32_add(swap16_add(sum));
        float inv = __builtin_amdgcn_rcpf(sum);
        s8 p12 = cat8(ck4(sv[1] * inv), ck4(sv[2] * inv)); s4 p0 = ck4(sv[0] * inv);
        __builtin_amdgcn_sched_barrier(0);
        vgrp(6, 8); __builtin_amdgcn_sched_barrier(0); vgrp(8, 10); __builtin_amdgcn_sched_barrier(0); vgrp(10, 12); __builtin_amdgcn_sched_barrier(0); vgrp(12, 14);
        const f4 z = {0.f, 0.f, 0.f, 0.f};
        f4 o = MFMA32(cat8(vx1, vx2), p12, z);
        s4 vx0 = ck4(va0 + va1);
        f4 o2 = MFMA16(vx0, p0, z);
        o += o2;
        const int hi = l15 >> 3;
        float va = hi ? o[2] : o[0], vb = hi ? o[3] : o[1];
        int c = 16 * wave + 4 * lg + 2 * hi;
        int cbh = c >= 56 ? 1 : 0, vp = (c - 56 * cbh) >> 1;
        u64* gp = (a.sy + SY_GP) + (long)par * 64 * 512 + ((l15 & 7) * 8 + 2 * jq + cbh) * 512 + h * 32 + vp;
        u32 qa = (__float_as_uint(va) + 0x80u) >> 8, qb = (__float_as_uint(vb) + 0x80u) >> 8;
        __hip_atomic_store((GAS u64*)gp, ((u64)((tag << 16) | (qa >> 8)) << 32) | ((qa << 24) | qb), RLX);
      }
      STAMP(4);
      asm volatile("s_barrier" ::: "memory");
      STAMP(5);
      vx1 = vx_tile(lds, wave, 8);
      STAMP(6);
      {
        const int tid = opq(threadIdx.x), l15 = tid & 15;
        u64* gp = (a.sy + SY_GP) + (long)par * 64 * 512 + wg * 512 + l15 * 32 + (tid >> 4);
        u64 x;
        for (int spins = 0;; ++spins) {
          x = __hip_atomic_load((GAS u64*)gp, RLX);
          if ((u32)(x >> 48) == tag) break;
          if (spins > SPIN_CAP) { __hip_atomic_store((GAS u32*)(a.sy + SY_ERR), 1u, RLX); break; }
          if ((spins & 255) == 255 && __hip_atomic_load((GAS u32*)(a.sy + SY_ERR), RLX)) break;
        }
        float a0 = __uint_as_float((u32)(x >> 24) << 8), a1 = __uint_as_float((u32)x << 8);
        a0 += dppf<0x128>(a0); a1 += dppf<0x128>(a1);
        a0 += dppf<0x124>(a0); a1 += dppf<0x124>(a1);
        a0 += dppf<0x4E>(a0); a1 += dppf<0x4E>(a1);
        a0 += dppf<0xB1>(a0); a1 += dppf<0xB1>(a1);
        int go = (wg >> 3) * 224 + (wg & 7) * 28 + (tid >> 4);
        if (l15 < 9) put_gran(l15 < 8 ? (a.sy + SY_MEMR) + (long)((par ^ 1) * 8 + l15) * 1792 + go : (a.sy + SY_MEMG) + (long)(s + 1) * 1792 + go, tag + 1, cvt2(a0, a1));
      }
      STAMP(7);
      vx2 = vx_tile(lds, wave, 24);
    } else {
      load_xseg(a.x, s + 1, lds, lane);
      asm volatile("s_waitcnt vmcnt(0)\n\ts_barrier" ::: "memory");
    }
  }
}

__global__ __launch_bounds__(512, 2) void chain_kernel(ChainArgs a) {
  extern __shared__ __attribute__((aligned(16))) char lds[];
  chain_body(a, lds, blockIdx.x);
}

constexpr int T_H = 0, T_S = 71680, T_END = 71680 + 81920;
__device__ __forceinline__ void tail1_item(const ChainArgs a, u16* __restrict__ Rp, int sp, int h, char* lds) {
  const int tid = threadIdx.x, lane = tid & 63, wave = tid >> 6;
  const int l15 = lane & 15, lg = lane >> 4;
  __syncthreads();
  if (wave < 7) {
#pragma unroll
    for (int qq = 0; qq < 8; ++qq) {
      int q = wave * 8 + qq, sg = q / 28, q2 = q % 28;
      int p = q2 * 64 + lane, row = p / 56, pos = p % 56;
      int ch = (pos & ~7) | ((pos & 7) ^ (row & 7));
      const u16* src = a.x + (long)(64 * sp + 32 * sg + row) * DM + ch * 8;
      __builtin_amdgcn_global_load_lds((const GAS void*)src, (LAS void*)(lds + T_H + sg * 35840 + 7168 + q2 * 1024), 16, 0, 0);
    }
  }
  if (tid < 448) {
#pragma unroll
    for (int sg = 0; sg < 2; ++sg) {
      int s = 2 * sp + sg;
      if (s == 0) { int r = tid / 56, c8 = tid % 56; *(u4v*)(lds + T_H + hoff(r, c8 * 8)) = *(const u4v*)(a.mem0 + r * DM + c8 * 8); }
      else {
        u64* gm = (a.sy + SY_MEMG) + (long)s * 1792 + tid;
        u64* p[4] = {gm, gm + 448, gm + 896, gm + 1344}; u32 v[4];
        get_gran<4>(p, s + 1, v, (u32*)(a.sy + SY_ERR));
        int hi = tid >= 224 ? 1 : 0, cp = tid - 224 * hi;
#pragma unroll
        for (int k = 0; k < 4; ++k) *(u32*)(lds + T_H + sg * 35840 + hoff(2 * k + hi, 2 * cp)) = v[k];
      }
    }
  }
  asm volatile("s_waitcnt vmcnt(0)" ::: "memory");
  __syncthreads();
  f4 acc[4][4];
#pragma unroll
  for (int t = 0; t < 4; ++t)
#pragma unroll
    for (int u = 0; u < 4; ++u) acc[t][u] = f4{0.f, 0.f, 0.f, 0.f};
  {
    const u16* W = a.At + (long)h * DM * DM + (long)(16 * 3 * wave + l15) * DM + 8 * lg;
    const u16* W3 = a.At + (long)h * DM * DM + (long)(16 * (24 + (wave >> 1)) + l15) * DM + 32 * 7 * (wave & 1) + 8 * lg;
#pragma unroll 2
    for (int ks = 0; ks < 14; ++ks) {
      bf8 w0 = *(const bf8*)(W + 32 * ks), w1 = *(const bf8*)(W + 16 * DM + 32 * ks), w2 = *(const bf8*)(W + 32 * DM + 32 * ks);
#pragma unroll
      for (int u = 0; u < 4; ++u) {
        bf8 xf = *(const bf8*)(lds + T_H + (u >> 1) * 35840 + hoff(8 + 16 * (u & 1) + l15, 32 * ks + 8 * lg));
        acc[0][u] = __builtin_amdgcn_mfma_f32_16x16x32_bf16(w0, xf, acc[0][u], 0, 0, 0);
        acc[1][u] = __builtin_amdgcn_mfma_f32_16x16x32_bf16(w1, xf, acc[1][u], 0, 0, 0);
        acc[2][u] = __builtin_amdgcn_mfma_f32_16x16x32_bf16(w2, xf, acc[2][u], 0, 0, 0);
      }
    }
#pragma unroll 1
    for (int i = 0; i < 7; ++i) {
      bf8 w3 = *(const bf8*)(W3 + 32 * i);
      int k2 = 7 * (wave & 1) + i;
#pragma unroll
      for (int u = 0; u < 4; ++u) {
        bf8 xf = *(const bf8*)(lds + T_H + (u >> 1) * 35840 + hoff(8 + 16 * (u & 1) + l15, 32 * k2 + 8 * lg));
        acc[3][u] = __builtin_amdgcn_mfma_f32_16x16x32_bf16(w3, xf, acc[3][u], 0, 0, 0);
      }
    }
  }
  {
    const int c0 = 16 * (3 * wave) + 4 * lg, c3 = 16 * (24 + (wave >> 1)) + 4 * lg;
#pragma unroll
    for (int u = 0; u < 4; ++u) {
      s8 tb0 = cat8(pk4(acc[0][u]), pk4(acc[1][u])), tb1 = cat8(pk4(acc[2][u]), pk4(acc[3][u]));
      const char* hb = lds + T_H + (u >> 1) * 35840;
#pragma unroll
      for (int kt = 0; kt < 3; ++kt) {
        int row = KB2[kt] + l15;
        s4 h0 = *(const s4*)(hb + hoff(row, c0)), h1 = *(const s4*)(hb + hoff(row, c0 + 16));
        s4 h2 = *(const s4*)(hb + hoff(row, c0 + 32)), h3 = *(const s4*)(hb + hoff(row, c3));
        f4 sa = {0.f, 0.f, 0.f, 0.f};
        sa = MFMA32(cat8(h0, h1), tb0, sa);
        sa = MFMA32(cat8(h2, h3), tb1, sa);
        if (kt < 2 || lg >= 2) {
          int gi = kt < 2 ? 4 * kt + lg : 6 + lg;
          *(f4*)(lds + T_S + (((wave * 4 + u) * 10 + gi) * 16 + l15) * 16) = sa;
        }
      }
    }
  }
  __syncthreads();
  {
    const int u = wave & 3, chalf = wave >> 2;
    const char* hb = lds + T_H + (u >> 1) * 35840;
    f4 sv[3];
    const int tq = 16 * (u & 1) + l15;
#pragma unroll
    for (int kt = 0; kt < 3; ++kt) {
      int gi = kt < 2 ? 4 * kt + lg : 8 + (lg & 1);
      const char* sp2 = lds + T_S + ((u * 10 + gi) * 16 + l15) * 16;
      f4 v0 = *(const f4*)(sp2) + *(const f4*)(sp2 + 10240), v1 = *(const f4*)(sp2 + 20480) + *(const f4*)(sp2 + 30720);
      f4 v2 = *(const f4*)(sp2 + 40960) + *(const f4*)(sp2 + 51200), v3 = *(const f4*)(sp2 + 61440) + *(const f4*)(sp2 + 71680);
      f4 v = ((v0 + v1) + (v2 + v3)) * 0.0625f;
#pragma unroll
      for (int r = 0; r < 4; ++r) {
        int kk = KB2[kt] + 4 * lg + r;
        bool ok = (kt < 2 || lg >= 2) && (kk - 8 <= tq);
        sv[kt][r] = ok ? v[r] : -INFINITY;
      }
    }
    float mx = -INFINITY;
#pragma unroll
    for (int kt = 0; kt < 3; ++kt)
#pragma unroll
      for (int r = 0; r < 4; ++r) mx = fmaxf(mx, sv[kt][r]);
    mx = fmaxf(mx, __shfl_xor(mx, 16)); mx = fmaxf(mx, __shfl_xor(mx, 32));
    float sum = 0.f;
#pragma unroll
    for (int kt = 0; kt < 3; ++kt)
#pragma unroll
      for (int r = 0; r < 4; ++r) { float p = __expf(sv[kt][r] - mx); sv[kt][r] = p; sum += p; }
    sum += __shfl_xor(sum, 16); sum += __shfl_xor(sum, 32);
    float inv = 1.f / sum;
    s8 p01 = cat8(pk4(sv[0] * inv), pk4(sv[1] * inv)); s4 p2 = pk4(sv[2] * inv);
    const int q4 = l15 >> 2, p4 = lane & 3;
    u16* rp = Rp + (long)(64 * sp + 16 * u + l15) * (NH * DM) + h * DM + 4 * lg;
#pragma unroll
    for (int v = 0; v < 14; ++v) {
      int ct = 14 * chalf + v, cc = 16 * ct + 4 * p4;
      s4 t0 = tr_read(hb, hoff(4 * lg + q4, cc)), t1 = tr_read(hb, hoff(16 + 4 * lg + q4, cc));
      s4 t2 = tr_read(hb, hoff(24 + 4 * lg + q4, cc));
      const f4 z = {0.f, 0.f, 0.f, 0.f};
      f4 r = MFMA32(cat8(t0, t1), p01, z) + MFMA16(t2, p2, z);
      u2v o = {pack2(r[0], r[1]), pack2(r[2], r[3])};
      *(u2v*)(rp + 16 * ct) = o;
    }
  }
}
__global__ __launch_bounds__(512, 2) void tail1_kernel(ChainArgs a, u16* Rp) {
  extern __shared__ __attribute__((aligned(16))) char lds[];
  tail1_item(a, Rp, blockIdx.x >> 4, blockIdx.x & 15, lds);
}
static inline GemmArgs mk(const u16* A, const u16* B, u16* C, long sA, long sB, long sC, int lda, int ldb, int ldc, int M, int N, int K) {
  GemmArgs g; g.A = A; g.B = B; g.C = C; g.sA = sA; g.sB = sB; g.sC = sC; g.lda = lda; g.ldb = ldb; g.ldc = ldc; g.M = M; g.N = N; g.K = K; return g;
}
extern "C" void kernel_launch(void* const* d_in, const int* in_sizes, int n_in, void* d_out, int out_size, void* d_ws, size_t ws_size,
                              hipStream_t stream) {
  const u16* Ax = (const u16*)d_in[0]; const u16* mem0 = (const u16*)d_in[1];
  const u16* Wq = (const u16*)d_in[2]; const u16* Wk = (const u16*)d_in[3]; const u16* Wv = (const u16*)d_in[4]; const u16* Wo = (const u16*)d_in[5];
  const u16* Bx = (const u16*)d_in[7]; const u16* ckv = (const u16*)d_in[8];
  const u16* wq = (const u16*)d_in[9]; const u16* wuk = (const u16*)d_in[10]; const u16* wuv = (const u16*)d_in[11]; const u16* wo = (const u16*)d_in[12];
  u16* out = (u16*)d_out;
  char* ws = (char*)d_ws;
  size_t off = 0;
  auto alloc = [&](size_t bytes) { char* p = ws + off; off += (bytes + 255) & ~(size_t)255; return p; };
  u64* sy = (u64*)alloc((size_t)SY_END * 8);
  size_t sync_bytes = off;
  const size_t big = (size_t)S_ * HD * 2;
  u16* At = (u16*)alloc((size_t)NH * DM * DM * 2); u16* Bs = (u16*)alloc((size_t)NH * DM * DM * 2);
  u16* Rp = (u16*)alloc((size_t)S_ * NH * DM * 2);
  u16* Qb = (u16*)alloc(big); u16* Kb = (u16*)alloc(big); u16* Vb = (u16*)alloc(big); u16* Ctx = (u16*)alloc(big);

  (void)hipMemsetAsync(ws, 0, sync_bytes, stream);
  gemm_kernel<64, false, false><<<dim3(7, 4, 16), 256, 0, stream>>>(mk(Wk, Wq, At, 256, 256, (long)DM * DM, HD, HD, DM, DM, DM, 256));
  gemm_kernel<64, true, true><<<dim3(7, 4, 16), 256, 0, stream>>>(mk(Wv, Wo, Bs, 256, (long)256 * DM, DM, HD, DM, NH * DM, DM, DM, 256));
  ChainArgs ca; ca.x = Ax; ca.mem0 = mem0; ca.At = At; ca.Bs = Bs; ca.sy = sy;
  (void)hipFuncSetAttribute((const void*)chain_kernel, hipFuncAttributeMaxDynamicSharedMemorySize, L_END);
  chain_kernel<<<64, 512, L_END, stream>>>(ca);
  (void)hipFuncSetAttribute((const void*)tail1_kernel, hipFuncAttributeMaxDynamicSharedMemorySize, T_END);
  tail1_kernel<<<512, 512, T_END, stream>>>(ca, Rp);
  gemm_kernel<64, false, false><<<dim3(7, 16, 1), 256, 0, stream>>>(mk(Rp, Bs, out, 0, 0, 0, NH * DM, NH * DM, 896, S_, DM, NH * DM));
  gemm_kernel<128, true, false><<<dim3(32, 16, 1), 256, 0, stream>>>(mk(Bx, wq, Qb, 0, 0, 0, DM, HD, HD, S_, HD, DM));
  gemm_kernel<128, true, false><<<dim3(32, 16, 1), 256, 0, stream>>>(mk(ckv, wuk, Kb, 0, 0, 0, DC, HD, HD, S_, HD, DC));
  gemm_kernel<128, true, false><<<dim3(32, 16, 1), 256, 0, stream>>>(mk(ckv, wuv, Vb, 0, 0, 0, DC, HD, HD, S_, HD, DC));
  (void)hipFuncSetAttribute((const void*)attnB_kernel, hipFuncAttributeMaxDynamicSharedMemorySize, 131072);
  attnB_kernel<<<256, 256, 131072, stream>>>(Qb, Kb, Vb, Ctx);
  gemm_kernel<64, true, false><<<dim3(7, 16, 1), 256, 0, stream>>>(mk(Ctx, wo, out + 448, 0, 0, 0, HD, DM, 896, S_, DM, HD));
}
```

```cpp
#include <hip/hip_runtime.h>
#include <hip/hip_bf16.h>
#include <stdint.h>

typedef unsigned short u16;
typedef unsigned int u32;
typedef unsigned long long u64;
typedef __bf16 bf8 __attribute__((ext_vector_type(8)));
typedef short s4 __attribute__((ext_vector_type(4)));
typedef short s8 __attribute__((ext_vector_type(8)));
typedef float f4 __attribute__((ext_vector_type(4)));
typedef float f16v __attribute__((ext_vector_type(16)));
typedef u32 u4v __attribute__((ext_vector_type(4)));
typedef u32 u2v __attribute__((ext_vector_type(2)));
#define LAS __attribute__((address_space(3)))
#define GAS __attribute__((address_space(1)))

static __device__ __forceinline__ u16 f2bf(float f) {
  u32 u = __float_as_uint(f);
  u += 0x7fffu + ((u >> 16) & 1u);
  return (u16)(u >> 16);
}
static __device__ __forceinline__ float bf2f(u16 x) { return __uint_as_float(((u32)x) << 16); }
static __device__ __forceinline__ u32 pack2(float a, float b) { return (u32)f2bf(a) | ((u32)f2bf(b) << 16); }
static __device__ __forceinline__ bf8 as_bf8(u4v v) { return __builtin_bit_cast(bf8, v); }
static __device__ __forceinline__ s4 tr_read(const char* lds_base, unsigned off) {
  return __builtin_amdgcn_ds_read_tr16_b64_v4i16((LAS s4*)(lds_base + off));
}

constexpr int S_ = 2048, DM = 448, NH = 16, DH = 256, HD = 4096, MEM = 8, SEG = 32, NSEG = 64, DC = 1024;

struct GemmArgs {
  const u16* A; const u16* B; u16* C;
  long sA, sB, sC;
  int lda, ldb, ldc;
  int M, N, K;
};
__device__ __forceinline__ unsigned off_b(unsigned row, unsigned ch) { return 256u * row + 16u * (ch ^ (((row & 3) << 2) | ((row >> 2) & 3))); }

template <int BN, bool B_KN, bool TRANS_OUT>
__global__ __launch_bounds__(256, 2) void gemm_kernel(GemmArgs g) {
  __shared__ __attribute__((aligned(16))) char lds[32768];
  char* ldsA = lds; char* ldsB = lds + 16384;
  const int tid = threadIdx.x, lane = tid & 63, wave = tid >> 6;
  const int wm = wave >> 1, wn = wave & 1;
  const int m0 = blockIdx.y * 128, n0 = blockIdx.x * BN;
  const u16* A = g.A + (long)blockIdx.z * g.sA;
  const u16* B = g.B + (long)blockIdx.z * g.sB;
  u16* C = g.C + (long)blockIdx.z * g.sC;
  constexpr int NT = BN / 32;
  constexpr int BCH = BN * 64 / 8 / 256;
  f4 acc[4][NT];
#pragma unroll
  for (int i = 0; i < 4; ++i)
#pragma unroll
    for (int j = 0; j < NT; ++j) acc[i][j] = f4{0.f, 0.f, 0.f, 0.f};
  u4v ra[4], rb[BCH];
  auto load_regs = [&](int k0) {
#pragma unroll
    for (int i = 0; i < 4; ++i) {
      int id = tid + 256 * i, row = id >> 3, ch = id & 7;
      int gr = m0 + row; gr = gr < g.M ? gr : g.M - 1;
      ra[i] = *(const u4v*)(A + (long)gr * g.lda + k0 + ch * 8);
    }
#pragma unroll
    for (int i = 0; i < BCH; ++i) {
      int id = tid + 256 * i;
      if (B_KN) {
        int row = id / (BN / 8), ch = id % (BN / 8);
        int gn = n0 + ch * 8; gn = gn < g.N ? gn : g.N - 8;
        rb[i] = *(const u4v*)(B + (long)(k0 + row) * g.ldb + gn);
      } else {
        int row = id >> 3, ch = id & 7;
        int gn = n0 + row; gn = gn < g.N ? gn : g.N - 1;
        rb[i] = *(const u4v*)(B + (long)gn * g.ldb + k0 + ch * 8);
      }
    }
  };
  auto store_lds = [&]() {
#pragma unroll
    for (int i = 0; i < 4; ++i) {
      int id = tid + 256 * i, row = id >> 3, ch = id & 7;
      *(u4v*)(ldsA + row * 128 + ((ch ^ (row & 7)) << 4)) = ra[i];
    }
#pragma unroll
    for (int i = 0; i < BCH; ++i) {
      int id = tid + 256 * i;
      if (B_KN) {
        int row = id / (BN / 8), ch = id % (BN / 8);
        *(u4v*)(ldsB + off_b(row, ch)) = rb[i];
      } else {
        int row = id >> 3, ch = id & 7;
        *(u4v*)(ldsB + row * 128 + ((ch ^ (row & 7)) << 4)) = rb[i];
      }
    }
  };
  const int nk = g.K / 64;
  load_regs(0);
  for (int kt = 0; kt < nk; ++kt) {
    __syncthreads();
    store_lds();
    __syncthreads();
    if (kt + 1 < nk) load_regs((kt + 1) * 64);
#pragma unroll
    for (int ks = 0; ks < 2; ++ks) {
      bf8 af[4], bfr[NT];
#pragma unroll
      for (int i = 0; i < 4; ++i) {
        int row = wm * 64 + i * 16 + (lane & 15), ch = ks * 4 + (lane >> 4);
        af[i] = *(const bf8*)(ldsA + row * 128 + ((ch ^ (row & 7)) << 4));
      }
#pragma unroll
      for (int j = 0; j < NT; ++j) {
        if (B_KN) {
          int c = wn * NT + j;
          unsigned gq = lane >> 4, q = (lane & 15) >> 2, p = lane & 3;
          s4 lo = tr_read(ldsB, off_b(32 * ks + 8 * gq + q, 2 * c + (p >> 1)) + 8 * (p & 1));
          s4 hi = tr_read(ldsB, off_b(32 * ks + 8 * gq + 4 + q, 2 * c + (p >> 1)) + 8 * (p & 1));
          s8 v = {lo[0], lo[1], lo[2], lo[3], hi[0], hi[1], hi[2], hi[3]};
          bfr[j] = __builtin_bit_cast(bf8, v);
        } else {
          int row = wn * (BN / 2) + j * 16 + (lane & 15), ch = ks * 4 + (lane >> 4);
          bfr[j] = *(const bf8*)(ldsB + row * 128 + ((ch ^ (row & 7)) << 4));
        }
      }
#pragma unroll
      for (int i = 0; i < 4; ++i)
#pragma unroll
        for (int j = 0; j < NT; ++j) {
          if (TRANS_OUT) acc[i][j] = __builtin_amdgcn_mfma_f32_16x16x32_bf16(af[i], bfr[j], acc[i][j], 0, 0, 0);
          else acc[i][j] = __builtin_amdgcn_mfma_f32_16x16x32_bf16(bfr[j], af[i], acc[i][j], 0, 0, 0);
        }
    }
  }
#pragma unroll
  for (int i = 0; i < 4; ++i)
#pragma unroll
    for (int j = 0; j < NT; ++j) {
      u2v v = {pack2(acc[i][j][0], acc[i][j][1]), pack2(acc[i][j][2], acc[i][j][3])};
      if (TRANS_OUT) {
        int m = m0 + wm * 64 + i * 16 + 4 * (lane >> 4), n = n0 + wn * (BN / 2) + j * 16 + (lane & 15);
        if (m < g.M && n < g.N) *(u2v*)(C + (long)n * g.ldc + m) = v;
      } else {
        int m = m0 + wm * 64 + i * 16 + (lane & 15), n = n0 + wn * (BN / 2) + j * 16 + 4 * (lane >> 4);
        if (m < g.M && n < g.N) *(u2v*)(C + (long)m * g.ldc + n) = v;
      }
    }
}
__device__ __forceinline__ void st16_sc1(void* p, u4v v) { asm volatile("global_store_dwordx4 %0, %1, off sc1\n\ts_nop 2" :: "v"(p), "v"(v) : "memory"); }
__device__ __forceinline__ void st8_sc1(void* p, u2v v) { asm volatile("global_store_dwordx2 %0, %1, off sc1\n\ts_nop 2" :: "v"(p), "v"(v) : "memory"); }
typedef __bf16 bf2 __attribute__((ext_vector_type(2)));
__device__ __forceinline__ u32 cvt2(float a, float b) { bf2 r = {(__bf16)a, (__bf16)b}; return __builtin_bit_cast(u32, r); }
template <int BM, bool B_KN, int EPI>
__device__ __forceinline__ void gemm_item(char* lds, const u16* __restrict__ A, int lda, const u16* __restrict__ B, int ldb, int nk,
                                          u16* __restrict__ C, int ldc, int nvalid) {
  constexpr int BN = 128, WN = BM == 256 ? 2 : 4, NT = BN / WN / 16, AI = BM / 64, SA = BM * 128, STG = SA + 16384;
  const int tid = threadIdx.x, lane = tid & 63, wave = __builtin_amdgcn_readfirstlane(tid >> 6);
  const int wm = wave / WN, wn = wave % WN, l15 = lane & 15, lg = lane >> 4;
  unsigned ao, bo;
  { int row = wave * AI * 8 + (lane >> 3); ao = (unsigned)(row * lda + (((lane & 7) ^ (row & 7)) << 3)) * 2u; }
  if (B_KN) {
    int row = wave * 8 + (lane >> 4);
    int ch = (lane & 15) ^ (((row & 3) << 2) | ((row >> 2) & 3));
    bo = (unsigned)(row * ldb) * 2u;
    (void)ch;
  } else { int row = wave * 16 + (lane >> 3); bo = (unsigned)(row * ldb + (((lane & 7) ^ (row & 7)) << 3)) * 2u; }
  unsigned bo2 = 0;
  if (B_KN) {
    int r0 = wave * 8 + (lane >> 4), r1 = r0 + 4;
    int c0 = (lane & 15) ^ (((r0 & 3) << 2) | ((r0 >> 2) & 3)), c1 = (lane & 15) ^ (((r1 & 3) << 2) | ((r1 >> 2) & 3));
    if (EPI == 2) { c0 = c0 * 8 < nvalid ? c0 : 0; c1 = c1 * 8 < nvalid ? c1 : 0; }
    bo = (unsigned)(r0 * ldb + c0 * 8) * 2u; bo2 = (unsigned)(r1 * ldb + c1 * 8) * 2u;
  } else bo2 = bo + (unsigned)(8 * ldb) * 2u;
  auto issue = [&](int kt, int buf) {
    char* sa = lds + buf * STG + wave * AI * 1024; char* sb = lds + buf * STG + SA + wave * 2048;
    const char* ga = (const char*)A + kt * 128 + ao;
#pragma unroll
    for (int i = 0; i < AI; ++i)
      __builtin_amdgcn_global_load_lds((const GAS void*)(ga + (long)i * 16 * lda), (LAS void*)(sa + i * 1024), 16, 0, 0);
    const char* gb = (const char*)B + (B_KN ? (long)kt * 128 * ldb : (long)kt * 128);
    __builtin_amdgcn_global_load_lds((const GAS void*)(gb + bo), (LAS void*)(sb), 16, 0, 0);
    __builtin_amdgcn_global_load_lds((const GAS void*)(gb + bo2), (LAS void*)(sb + 1024), 16, 0, 0);
  };
  f4 acc[4][NT];
#pragma unroll
  for (int i = 0; i < 4; ++i)
#pragma unroll
    for (int j = 0; j < NT; ++j) acc[i][j] = f4{0.f, 0.f, 0.f, 0.f};
  __syncthreads();
  issue(0, 0);
  if (nk > 1) issue(1, 1);
  int buf = 0;
  for (int kt = 0; kt < nk; ++kt) {
    if (kt + 1 < nk) { if (AI == 4) asm volatile("s_waitcnt vmcnt(6)" ::: "memory"); else asm volatile("s_waitcnt vmcnt(4)" ::: "memory"); }
    else asm volatile("s_waitcnt vmcnt(0)" ::: "memory");
    __builtin_amdgcn_s_barrier();
    asm volatile("" ::: "memory");
    if (kt + 2 < nk) issue(kt + 2, buf == 0 ? 2 : buf - 1);
    const char* sa = lds + buf * STG; const char* sb = sa + SA;
#pragma unroll
    for (int ks = 0; ks < 2; ++ks) {
      bf8 af[4], bfr[NT];
#pragma unroll
      for (int i = 0; i < 4; ++i) {
        int row = wm * 64 + i * 16 + l15, ch = ks * 4 + lg;
        af[i] = *(const bf8*)(sa + row * 128 + ((ch ^ (row & 7)) << 4));
      }
#pragma unroll
      for (int j = 0; j < NT; ++j) {
        if (B_KN) {
          int c = wn * NT + j;
          unsigned q = l15 >> 2, p = lane & 3;
          s4 lo = tr_read(sb, off_b(32 * ks + 8 * lg + q, 2 * c + (p >> 1)) + 8 * (p & 1));
          s4 hi = tr_read(sb, off_b(32 * ks + 8 * lg + 4 + q, 2 * c + (p >> 1)) + 8 * (p & 1));
          s8 v = {lo[0], lo[1], lo[2], lo[3], hi[0], hi[1], hi[2], hi[3]};
          bfr[j] = __builtin_bit_cast(bf8, v);
        } else {
          int row = wn * NT * 16 + j * 16 + l15, ch = ks * 4 + lg;
          bfr[j] = *(const bf8*)(sb + row * 128 + ((ch ^ (row & 7)) << 4));
        }
      }
#pragma unroll
      for (int i = 0; i < 4; ++i)
#pragma unroll
        for (int j = 0; j < NT; ++j) {
          if (EPI == 1) acc[i][j] = __builtin_amdgcn_mfma_f32_16x16x32_bf16(af[i], bfr[j], acc[i][j], 0, 0, 0);
          else acc[i][j] = __builtin_amdgcn_mfma_f32_16x16x32_bf16(bfr[j], af[i], acc[i][j], 0, 0, 0);
        }
    }
    buf = buf == 2 ? 0 : buf + 1;
  }
  __syncthreads();
  if (EPI == 1) {
    constexpr int P = BM * 2 + 16;
#pragma unroll
    for (int i = 0; i < 4; ++i)
#pragma unroll
      for (int j = 0; j < NT; ++j) {
        int n = wn * NT * 16 + j * 16 + l15, pos = wm * 64 + (i >> 1) * 32 + 8 * lg + 4 * (i & 1);
        *(u2v*)(lds + n * P + pos * 2) = u2v{cvt2(acc[i][j][0], acc[i][j][1]), cvt2(acc[i][j][2], acc[i][j][3])};
      }
    __syncthreads();
    constexpr int CPR = BM / 8;
#pragma unroll
    for (int it = 0; it < 128 * CPR / 512; ++it) {
      int id = it * 512 + tid, n = id / CPR, ch = id % CPR;
      st16_sc1(C + (long)n * ldc + ch * 8, *(const u4v*)(lds + n * P + ch * 16));
    }
  } else {
    constexpr int P = 256 + 16;
#pragma unroll
    for (int i = 0; i < 4; ++i)
#pragma unroll
      for (int j = 0; j < NT; ++j) {
        int m = wm * 64 + i * 16 + l15, n = wn * NT * 16 + j * 16 + 4 * lg;
        *(u2v*)(lds + m * P + n * 2) = u2v{cvt2(acc[i][j][0], acc[i][j][1]), cvt2(acc[i][j][2], acc[i][j][3])};
      }
    __syncthreads();
#pragma unroll
    for (int it = 0; it < BM * 16 / 512; ++it) {
      int id = it * 512 + tid, m = id >> 4, ch = id & 15;
      u4v v = *(const u4v*)(lds + m * P + ch * 16);
      if (EPI == 0) st16_sc1(C + (long)m * ldc + ch * 8, v);
      else if (ch * 8 < nvalid) *(u4v*)(C + (long)m * ldc + ch * 8) = v;
    }
  }
}
struct ChainArgs {
  const u16* x; const u16* mem0; const u16* At; const u16* Bs;
  u64* sy;
  u64* vm;
};
constexpr int SY_MEMG = 0, SY_GP = 64 * 1792, SY_MEMR = SY_GP + 2 * 64 * 512, SY_ERR = SY_MEMR + 2 * 8 * 1792, SY_FLV = SY_ERR + 512 + 64 * 384, SY_CNT = SY_FLV + 64 * 64, SY_END = SY_CNT + 128 * 32;
#define RLX __ATOMIC_RELAXED, __HIP_MEMORY_SCOPE_AGENT
constexpr int SPIN_CAP = 1 << 20;
__device__ __forceinline__ void put_gran(u64* p, u32 tag, u32 val) {
  __hip_atomic_store((GAS u64*)p, ((u64)tag << 32) | val, RLX);
}
template <int N>
__device__ __forceinline__ void get_gran(u64* const (&p)[N], u32 tag, u32 (&v)[N], u32* err) {
  for (int spins = 0;; ++spins) {
    bool ok = true;
#pragma unroll
    for (int k = 0; k < N; ++k) { u64 x = __hip_atomic_load((GAS u64*)p[k], RLX); v[k] = (u32)x; ok &= ((u32)(x >> 32) == tag); }
    if (ok) break;
    if (spins > SPIN_CAP) { __hip_atomic_store((GAS u32*)err, 1u, RLX); break; }
    if ((spins & 255) == 255 && __hip_atomic_load((GAS u32*)err, RLX)) break;
  }
}
__device__ __forceinline__ int opq(int v) { asm volatile("" : "+v"(v)); return v; }
__device__ __forceinline__ s8 cat8(s4 a, s4 b) { s8 v = {a[0], a[1], a[2], a[3], b[0], b[1], b[2], b[3]}; return v; }
__device__ __forceinline__ s4 pk4(f4 a) { u2v t = {pack2(a[0], a[1]), pack2(a[2], a[3])}; return __builtin_bit_cast(s4, t); }
#define MFMA32(a, b, c) __builtin_amdgcn_mfma_f32_16x16x32_bf16(__builtin_bit_cast(bf8, a), __builtin_bit_cast(bf8, b), c, 0, 0, 0)
#define MFMA16(a, b, c) __builtin_amdgcn_mfma_f32_16x16x16bf16_1k(a, b, c, 0, 0, 0)
__device__ __forceinline__ unsigned hoff(unsigned row, unsigned c) {
  unsigned ch = c >> 3;
  return row * 896u + (((ch & ~7u) | ((ch & 7u) ^ (row & 7u))) << 4) + (c & 7u) * 2u;
}
constexpr int KB2[3] = {0, 16, 24};
constexpr int KB3[3] = {0, 8, 24};
constexpr int L_B = 0;
constexpr int L_H = 100352;
constexpr int L_S = L_H + 35840;
constexpr int L_S2 = L_S;
constexpr int L_W = L_S + 10240;
constexpr int L_END = L_W + 16384;
#define STAMP(k)

__device__ __forceinline__ void load_xseg(const u16* x, int seg, char* lds, int lane) {
  lane = opq(lane);
  int lb = L_H + 7168; asm volatile("" : "+s"(lb));
  const char* xb = (const char*)(x + (long)32 * seg * DM);
  if (lane < 56) {
#pragma unroll
    for (int row = 0; row < 32; ++row) {
      const char* src = (xb + row * 896) + (unsigned)((lane ^ (row & 7)) << 4);
      __builtin_amdgcn_global_load_lds((const GAS void*)src, (LAS void*)(lds + lb + row * 896), 16, 0, 0);
    }
  }
}

__device__ __forceinline__ s4 ck4(f4 a) { u2v t = {cvt2(a[0], a[1]), cvt2(a[2], a[3])}; return __builtin_bit_cast(s4, t); }
template <int CTRL> __device__ __forceinline__ float dppf(float v) {
  return __int_as_float(__builtin_amdgcn_update_dpp(0, __float_as_int(v), CTRL, 0xf, 0xf, true));
}
__device__ __forceinline__ float swap16_max(float v) { auto r = __builtin_amdgcn_permlane16_swap(__float_as_uint(v), __float_as_uint(v), false, false); return fmaxf(__uint_as_float(r[0]), __uint_as_float(r[1])); }
__device__ __forceinline__ float swap32_max(float v) { auto r = __builtin_amdgcn_permlane32_swap(__float_as_uint(v), __float_as_uint(v), false, false); return fmaxf(__uint_as_float(r[0]), __uint_as_float(r[1])); }
__device__ __forceinline__ float swap16_add(float v) { auto r = __builtin_amdgcn_permlane16_swap(__float_as_uint(v), __float_as_uint(v), false, false); return __uint_as_float(r[0]) + __uint_as_float(r[1]); }
__device__ __forceinline__ float swap32_add(float v) { auto r = __builtin_amdgcn_permlane32_swap(__float_as_uint(v), __float_as_uint(v), false, false); return __uint_as_float(r[0]) + __uint_as_float(r[1]); }

__device__ __forceinline__ s4 vx_tile(const char* lds, int wave, int r0) {
  const int lane = opq(threadIdx.x) & 63, l15 = lane & 15, lg = lane >> 4;
  const unsigned r1 = L_H + (r0 + l15) * 896 + ((lg ^ (l15 & 7)) << 4), r2 = r1 ^ 64;
  const char* bp = lds + L_B + wave * 1024 + lane * 16;
  f4 a1 = {0.f, 0.f, 0.f, 0.f}, a2 = {0.f, 0.f, 0.f, 0.f};
#pragma unroll
  for (int ks = 0; ks < 14; ks += 2) {
    a1 = __builtin_amdgcn_mfma_f32_16x16x32_bf16(*(const bf8*)(lds + r1 + (ks >> 1) * 128), *(const bf8*)(bp + ks * 7168), a1, 0, 0, 0);
    a2 = __builtin_amdgcn_mfma_f32_16x16x32_bf16(*(const bf8*)(lds + r2 + (ks >> 1) * 128), *(const bf8*)(bp + (ks + 1) * 7168), a2, 0, 0, 0);
  }
  return ck4(a1 + a2);
}

__device__ __forceinline__ void chain_body(const ChainArgs a, char* lds, int wg) {
  const int tid = threadIdx.x, lane = tid & 63, wave = __builtin_amdgcn_readfirstlane(tid >> 6);
  const int h = wg >> 2, jq = wg & 3;
  const int l15 = lane & 15, lg = lane >> 4;
  if (wave == 7) load_xseg(a.x, 0, lds, lane);
  bf8 wf[3][14], wh[5];
  {
    const u16* W = a.At + (long)h * DM * DM;
#pragma unroll
    for (int t = 0; t < 3; ++t)
#pragma unroll
      for (int ks = 0; ks < 14; ++ks) wf[t][ks] = *(const bf8*)(W + (long)(16 * (3 * wave + t) + l15) * DM + 32 * ks + 8 * lg);
#pragma unroll
    for (int i = 0; i < 7; ++i) {
      bf8 t = *(const bf8*)(W + (long)(16 * (24 + (wave >> 1)) + l15) * DM + 32 * (7 * (wave & 1) + i) + 8 * lg);
      if (i < 5) wh[i] = t; else *(bf8*)(lds + L_W + (wave * 2 + i - 5) * 1024 + lane * 16) = t;
    }
  }
  for (int idx = wave; idx < 98; idx += 8) {
    int ks = idx / 7, nt = idx % 7;
    *(u4v*)(lds + L_B + idx * 1024 + lane * 16) = *(const u4v*)(a.Bs + (long)(h * DM + 112 * jq + 16 * nt + l15) * DM + 32 * ks + 8 * lg);
  }
  if (tid < 448) { int r = tid / 56, c8 = tid % 56; *(u4v*)(lds + L_H + hoff(r, c8 * 8)) = *(const u4v*)(a.mem0 + r * DM + c8 * 8); }
  asm volatile("s_waitcnt vmcnt(0)" ::: "memory");
  __syncthreads();
  s4 vx1 = {0, 0, 0, 0}, vx2 = {0, 0, 0, 0};
  if (wave < 7) { vx1 = vx_tile(lds, wave, 8); vx2 = vx_tile(lds, wave, 24); }
  for (int s = 0; s < NSEG - 1; ++s) {
    const int par = s & 1;
    const u32 tag = s + 1;
    if (s > 0 && tid < 448) {
      const int tid = opq(threadIdx.x);
      u64* gm = (a.sy + SY_MEMR) + (long)(par * 8 + (wg & 7)) * 1792 + tid;
      u64* p[4] = {gm, gm + 448, gm + 896, gm + 1344}; u32 v[4];
      get_gran<4>(p, tag, v, (u32*)(a.sy + SY_ERR));
      int hi = tid >= 224 ? 1 : 0, cp = tid - 224 * hi;
#pragma unroll
      for (int k = 0; k < 4; ++k) *(u32*)(lds + L_H + hoff(2 * k + hi, 2 * cp)) = v[k];
    }
    STAMP(0);
    __syncthreads();
    STAMP(1);
    if (s > 0 && tid == 448) put_gran(a.sy + SY_FLV + (s - 1) * 64 + wg, s, 0);
    {
      f4 acc[4];
#pragma unroll
      for (int t = 0; t < 4; ++t) acc[t] = f4{0.f, 0.f, 0.f, 0.f};
      const int lane0 = opq(threadIdx.x) & 63;
      const unsigned mo0 = L_H + (lane0 & 15) * 896 + (((lane0 >> 4) ^ (lane0 & 7)) << 4), mo1 = mo0 ^ 64;
      const unsigned e0 = (wave & 1) ? mo1 + 384 : mo0, e1 = (wave & 1) ? mo0 + 512 : mo1;
#pragma unroll
      for (int ks = 0; ks < 14; ++ks) {
        bf8 mf = *(const bf8*)(lds + ((ks & 1) ? mo1 : mo0) + (ks >> 1) * 128);
#pragma unroll
        for (int t = 0; t < 3; ++t) acc[t] = __builtin_amdgcn_mfma_f32_16x16x32_bf16(wf[t][ks], mf, acc[t], 0, 0, 0);
        if (ks < 7) {
          bf8 m2 = *(const bf8*)(lds + ((ks & 1) ? e1 : e0) + (ks >> 1) * 128);
          acc[3] = __builtin_amdgcn_mfma_f32_16x16x32_bf16(ks < 5 ? wh[ks < 5 ? ks : 0] : *(const bf8*)(lds + L_W + (wave * 2 + ks - 5) * 1024 + lane0 * 16), m2, acc[3], 0, 0, 0);
        }
      }
      s8 tb0 = cat8(ck4(acc[0]), ck4(acc[1])), tb1 = cat8(ck4(acc[2]), ck4(acc[3]));
      const int lane = opq(threadIdx.x) & 63, l15 = lane & 15, lg = lane >> 4;
      const int c0 = 16 * (3 * wave) + 4 * lg, c3 = 16 * (24 + (wave >> 1)) + 4 * lg;
#pragma unroll
      for (int kt = 0; kt < 3; ++kt) {
        int row = KB3[kt] + l15;
        s4 h0 = *(const s4*)(lds + L_H + hoff(row, c0)), h1 = *(const s4*)(lds + L_H + hoff(row, c0 + 16));
        s4 h2 = *(const s4*)(lds + L_H + hoff(row, c0 + 32)), h3 = *(const s4*)(lds + L_H + hoff(row, c3));
        f4 sa = {0.f, 0.f, 0.f, 0.f};
        sa = MFMA32(cat8(h0, h1), tb0, sa);
        sa = MFMA32(cat8(h2, h3), tb1, sa);
        if (l15 < 8 && (kt > 0 || lg < 2)) {
          int gi = kt == 0 ? lg : 4 * kt - 2 + lg;
          *(f4*)(lds + L_S + ((wave * 10 + gi) * 8 + l15) * 16) = sa;
        }
      }
    }
    STAMP(2);
    __syncthreads();
    STAMP(3);
    if (tid < 320) {
      const int tid = opq(threadIdx.x);
      const float* sp = (const float*)(lds + L_S) + tid;
      float v = ((sp[0] + sp[320]) + (sp[640] + sp[960])) + ((sp[1280] + sp[1600]) + (sp[1920] + sp[2240]));
      ((float*)(lds + L_S2))[tid] = v * 0.0625f;
    }
    asm volatile("s_waitcnt lgkmcnt(0)\n\ts_barrier" ::: "memory");
    STAMP(8);
    if (wave < 7) {
      {
        const int lane = opq(threadIdx.x) & 63, l15 = lane & 15, lg = lane >> 4;
        f4 sv[3];
#pragma unroll
        for (int kt = 0; kt < 3; ++kt) {
          int gi = kt == 0 ? (lg & 1) : 4 * kt - 2 + lg;
          sv[kt] = *(const f4*)(lds + L_S2 + (gi * 8 + (l15 & 7)) * 16);
        }
        f4 va0 = {0.f, 0.f, 0.f, 0.f}, va1 = {0.f, 0.f, 0.f, 0.f};
        const unsigned mo0 = L_H + l15 * 896 + ((lg ^ (l15 & 7)) << 4), mo1 = mo0 ^ 64;
        const char* bp = lds + L_B + wave * 1024 + lane * 16;
        auto vgrp = [&](int k0, int k1) {
#pragma unroll
          for (int ks = k0; ks < k1; ks += 2) {
            va0 = __builtin_amdgcn_mfma_f32_16x16x32_bf16(*(const bf8*)(lds + mo0 + (ks >> 1) * 128), *(const bf8*)(bp + ks * 7168), va0, 0, 0, 0);
            va1 = __builtin_amdgcn_mfma_f32_16x16x32_bf16(*(const bf8*)(lds + mo1 + (ks >> 1) * 128), *(const bf8*)(bp + (ks + 1) * 7168), va1, 0, 0, 0);
          }
        };
        vgrp(0, 2);
        if (lg >= 2) sv[0] = f4{-INFINITY, -INFINITY, -INFINITY, -INFINITY};
        float mx = -INFINITY;
#pragma unroll
        for (int kt = 0; kt < 3; ++kt)
#pragma unroll
          for (int r = 0; r < 4; ++r) mx = fmaxf(mx, sv[kt][r]);
        mx = swap32_max(swap16_max(mx));
        __builtin_amdgcn_sched_barrier(0);
        vgrp(2, 4);
        float sum = 0.f;
#pragma unroll
        for (int kt = 0; kt < 3; ++kt)
#pragma unroll
          for (int r = 0; r < 4; ++r) { float p = __expf(sv[kt][r] - mx); sv[kt][r] = p; sum += p; }
        __builtin_amdgcn_sched_barrier(0);
        vgrp(4, 6);
        sum = swap32_add(swap16_add(sum));
        float inv = __builtin_amdgcn_rcpf(sum);
        s8 p12 = cat8(ck4(sv[1] * inv), ck4(sv[2] * inv)); s4 p0 = ck4(sv[0] * inv);
        __builtin_amdgcn_sched_barrier(0);
        vgrp(6, 8); __builtin_amdgcn_sched_barrier(0); vgrp(8, 10); __builtin_amdgcn_sched_barrier(0); vgrp(10, 12); __builtin_amdgcn_sched_barrier(0); vgrp(12, 14);
        const f4 z = {0.f, 0.f, 0.f, 0.f};
        f4 o = MFMA32(cat8(vx1, vx2), p12, z);
        s4 vx0 = ck4(va0 + va1);
        f4 o2 = MFMA16(vx0, p0, z);
        o += o2;
        const int hi = l15 >> 3;
        float va = hi ? o[2] : o[0], vb = hi ? o[3] : o[1];
        int c = 16 * wave + 4 * lg + 2 * hi;
        int cbh = c >= 56 ? 1 : 0, vp = (c - 56 * cbh) >> 1;
        u64* gp = (a.sy + SY_GP) + (long)par * 64 * 512 + ((l15 & 7) * 8 + 2 * jq + cbh) * 512 + h * 32 + vp;
        u32 qa = (__float_as_uint(va) + 0x80u) >> 8, qb = (__float_as_uint(vb) + 0x80u) >> 8;
        __hip_atomic_store((GAS u64*)gp, ((u64)((tag << 16) | (qa >> 8)) << 32) | ((qa << 24) | qb), RLX);
        if (lg < 2) __hip_atomic_store((GAS u64*)(a.vm + ((long)(s * 64 + wg) * 7 + wave) * 32 + lane), __builtin_bit_cast(u64, vx0), RLX);
      }
      STAMP(4);
      asm volatile("s_barrier" ::: "memory");
      STAMP(5);
      vx1 = vx_tile(lds, wave, 8);
      STAMP(6);
      {
        const int tid = opq(threadIdx.x), l15 = tid & 15;
        u64* gp = (a.sy + SY_GP) + (long)par * 64 * 512 + wg * 512 + l15 * 32 + (tid >> 4);
        u64 x;
        for (int spins = 0;; ++spins) {
          x = __hip_atomic_load((GAS u64*)gp, RLX);
          if ((u32)(x >> 48) == tag) break;
          if (spins > SPIN_CAP) { __hip_atomic_store((GAS u32*)(a.sy + SY_ERR), 1u, RLX); break; }
          if ((spins & 255) == 255 && __hip_atomic_load((GAS u32*)(a.sy + SY_ERR), RLX)) break;
        }
        float a0 = __uint_as_float((u32)(x >> 24) << 8), a1 = __uint_as_float((u32)x << 8);
        a0 += dppf<0x128>(a0); a1 += dppf<0x128>(a1);
        a0 += dppf<0x124>(a0); a1 += dppf<0x124>(a1);
        a0 += dppf<0x4E>(a0); a1 += dppf<0x4E>(a1);
        a0 += dppf<0xB1>(a0); a1 += dppf<0xB1>(a1);
        int go = (wg >> 3) * 224 + (wg & 7) * 28 + (tid >> 4);
        if (l15 < 9) put_gran(l15 < 8 ? (a.sy + SY_MEMR) + (long)((par ^ 1) * 8 + l15) * 1792 + go : (a.sy + SY_MEMG) + (long)(s + 1) * 1792 + go, tag + 1, cvt2(a0, a1));
      }
      STAMP(7);
      vx2 = vx_tile(lds, wave, 24);
    } else {
      load_xseg(a.x, s + 1, lds, lane);
      asm volatile("s_waitcnt vmcnt(0)\n\ts_barrier" ::: "memory");
    }
  }
  {
    const int s = NSEG - 1, par = s & 1;
    if (tid < 448) {
      u64* gm = (a.sy + SY_MEMR) + (long)(par * 8 + (wg & 7)) * 1792 + tid;
      u64* p[4] = {gm, gm + 448, gm + 896, gm + 1344}; u32 v[4];
      get_gran<4>(p, s + 1, v, (u32*)(a.sy + SY_ERR));
      int hi = tid >= 224 ? 1 : 0, cp = tid - 224 * hi;
#pragma unroll
      for (int k = 0; k < 4; ++k) *(u32*)(lds + L_H + hoff(2 * k + hi, 2 * cp)) = v[k];
    }
    __syncthreads();
    if (tid == 448) put_gran(a.sy + SY_FLV + (s - 1) * 64 + wg, s, 0);
    if (wave < 7) {
      s4 vx0 = vx_tile(lds, wave, 0);
      if (lg < 2) __hip_atomic_store((GAS u64*)(a.vm + ((long)(s * 64 + wg) * 7 + wave) * 32 + lane), __builtin_bit_cast(u64, vx0), RLX);
    }
    asm volatile("s_waitcnt vmcnt(0)" ::: "memory");
    __syncthreads();
    if (tid == 448) put_gran(a.sy + SY_FLV + s * 64 + wg, s + 1, 0);
  }
}
__device__ __forceinline__ void attn_item(char* lds, const u16* __restrict__ Q, const u16* __restrict__ K,
                                          const u16* __restrict__ V, u16* __restrict__ O, int h, int qb) {
  const int tid = threadIdx.x, lane = tid & 63, wave = __builtin_amdgcn_readfirstlane(tid >> 6);
  const int q0 = qb * 256, qw = q0 + 32 * wave;
  const int hh = lane >> 5, l31 = lane & 31;
  const char* Kh = (const char*)(K + h * 256); const char* Vh = (const char*)(V + h * 256);
  bf8 qf[12];
  __syncthreads();
  {
    const u16* qp = Q + (long)(qw + l31) * HD + h * 256 + 8 * hh;
#pragma unroll
    for (int ks = 0; ks < 16; ++ks) {
      bf8 t = *(const bf8*)(qp + 16 * ks);
      if (ks < 12) qf[ks] = t; else *(bf8*)(lds + 131072 + wave * 4096 + (ks - 12) * 1024 + lane * 16) = t;
    }
  }
  auto issue = [&](int kt, int buf) {
    char* kb = lds + buf * 32768 + wave * 4096; char* vb = kb + 65536;
    const char* kg = Kh + (long)(kt * 64 + 8 * wave) * HD * 2; const char* vg = Vh + (long)(kt * 64 + 8 * wave) * HD * 2;
    const unsigned ln = (unsigned)opq(threadIdx.x) & 63u, h2 = ln >> 5, pos = ln & 31u;
    const unsigned ko = h2 * HD * 2 + ((pos ^ (8 * (wave & 1) + h2)) << 4), vo = h2 * HD * 2 + ((pos ^ (h2 << 2)) << 4);
#pragma unroll
    for (int i = 0; i < 4; ++i) {
      __builtin_amdgcn_global_load_lds((const GAS void*)(kg + i * 4 * HD + (ko ^ (unsigned)(i << 5))), (LAS void*)(kb + i * 1024), 16, 0, 0);
      __builtin_amdgcn_global_load_lds((const GAS void*)(vg + i * 4 * HD + (vo ^ (unsigned)((i & 1) << 7))), (LAS void*)(vb + i * 1024), 16, 0, 0);
    }
  };
  f16v o[8];
#pragma unroll
  for (int i = 0; i < 8; ++i)
#pragma unroll
    for (int j = 0; j < 16; ++j) o[i][j] = 0.f;
  float m = -INFINITY, lsum = 0.f;
  const float sc = 0.0625f * 1.4426950408889634f;
  const int nkt = (q0 + 256) / 64;
  issue(0, 0);
  for (int kt = 0; kt < nkt; ++kt) {
    asm volatile("s_waitcnt vmcnt(0)" ::: "memory");
    __builtin_amdgcn_s_barrier();
    asm volatile("" ::: "memory");
    if (kt + 1 < nkt) issue(kt + 1, (kt + 1) & 1);
#pragma unroll
    for (int st = 0; st < 2; ++st) {
      const int k0 = kt * 64 + 32 * st;
      if (k0 > qw + 31) continue;
      f16v s;
#pragma unroll
      for (int j = 0; j < 16; ++j) s[j] = 0.f;
      const unsigned ln = (unsigned)opq(threadIdx.x) & 63u;
      const unsigned ka = (kt & 1) * 32768 + (ln & 31u) * 512 + (((ln >> 5) ^ (ln & 15u)) << 4);
#pragma unroll
      for (int ks = 0; ks < 16; ++ks) {
        bf8 kf = *(const bf8*)(lds + (ka ^ (unsigned)((ks & 7) << 5)) + st * 16384 + (ks >> 3) * 256);
        s = __builtin_amdgcn_mfma_f32_32x32x16_bf16(kf, ks < 12 ? qf[ks < 12 ? ks : 0] : *(const bf8*)(lds + 131072 + wave * 4096 + (ks - 12) * 1024 + ln * 16), s, 0, 0, 0);
        if ((ks & 3) == 3) __builtin_amdgcn_sched_barrier(0);
      }
      float mx = -INFINITY;
      if (k0 + 31 > qw) {
#pragma unroll
        for (int j = 0; j < 16; ++j) { int key = k0 + (j & 3) + 8 * (j >> 2) + 4 * hh; if (key > qw + l31) s[j] = -INFINITY; }
      }
#pragma unroll
      for (int j = 0; j < 16; ++j) mx = fmaxf(mx, s[j]);
      mx = swap32_max(mx) * sc;
      float mn = fmaxf(m, mx);
      if (__builtin_amdgcn_ballot_w64(mn > m) != 0) {
        float alpha = __builtin_amdgcn_exp2f(m - mn);
        m = mn; lsum *= alpha;
#pragma unroll
        for (int i = 0; i < 8; ++i)
#pragma unroll
          for (int j = 0; j < 16; ++j) o[i][j] *= alpha;
      }
      float ps = 0.f;
#pragma unroll
      for (int j = 0; j < 16; ++j) { float p = __builtin_amdgcn_exp2f(s[j] * sc - mn); s[j] = p; ps += p; }
      lsum += ps;
      bf8 pf[2];
#pragma unroll
      for (int ss = 0; ss < 2; ++ss) {
        u4v t = {cvt2(s[8 * ss + 0], s[8 * ss + 1]), cvt2(s[8 * ss + 2], s[8 * ss + 3]),
                 cvt2(s[8 * ss + 4], s[8 * ss + 5]), cvt2(s[8 * ss + 6], s[8 * ss + 7])};
        pf[ss] = as_bf8(t);
      }
      const unsigned l2 = (unsigned)opq(threadIdx.x) & 63u, blk = (l2 >> 4) & 1, q4 = (l2 & 15) >> 2, p4 = l2 & 3;
      const unsigned va = 65536 + (kt & 1) * 32768 + (4 * (l2 >> 5) + q4) * 512 + (((2 * blk + (p4 >> 1)) ^ (q4 << 2)) << 4) + 8 * (p4 & 1);
#pragma unroll
      for (int dt = 0; dt < 8; ++dt) {
#pragma unroll
        for (int ss = 0; ss < 2; ++ss) {
          s4 lo = tr_read(lds, (va ^ (unsigned)((dt & 3) << 6)) + (dt >> 2) * 256 + st * 16384 + ss * 8192);
          s4 hi = tr_read(lds, (va ^ (unsigned)((dt & 3) << 6)) + (dt >> 2) * 256 + st * 16384 + ss * 8192 + 4096);
          s8 v = {lo[0], lo[1], lo[2], lo[3], hi[0], hi[1], hi[2], hi[3]};
          o[dt] = __builtin_amdgcn_mfma_f32_32x32x16_bf16(__builtin_bit_cast(bf8, v), pf[ss], o[dt], 0, 0, 0);
        }
        if (dt & 1) __builtin_amdgcn_sched_barrier(0);
      }
    }
  }
  lsum = swap32_add(lsum);
  float inv = 1.f / lsum;
  __syncthreads();
  char* ob = lds + wave * (32 * 528);
  {
  const int lane = opq(threadIdx.x) & 63, hh = lane >> 5, l31 = lane & 31;
#pragma unroll
  for (int dt = 0; dt < 8; ++dt)
#pragma unroll
    for (int g = 0; g < 4; ++g)
      *(u2v*)(ob + l31 * 528 + (32 * dt + 8 * g + 4 * hh) * 2) = u2v{cvt2(o[dt][4 * g] * inv, o[dt][4 * g + 1] * inv), cvt2(o[dt][4 * g + 2] * inv, o[dt][4 * g + 3] * inv)};
  asm volatile("s_waitcnt lgkmcnt(0)" ::: "memory");
  u16* op = O + (long)qw * HD + h * 256;
#pragma unroll
  for (int it = 0; it < 16; ++it) {
    int r = 2 * it + hh;
    st16_sc1(op + (long)r * HD + l31 * 8, *(const u4v*)(ob + r * 528 + l31 * 16));
  }
  }
}
constexpr int L_QW = 0;
enum { C_Q0 = 0, C_H = 8, C_C = 24, C_T = 32, C_V = 40, C_P = 48 };
__device__ __forceinline__ u32* cnt_ptr(u64* sy, int i) { return (u32*)(sy + SY_CNT + i * 32); }
__device__ __forceinline__ void wait_cnt(u64* sy, int ci, u32 need) {
  if (threadIdx.x < 64) {
    u32* c = cnt_ptr(sy, ci); u32* err = (u32*)(sy + SY_ERR);
    for (int spins = 0;; ++spins) {
      u32 v = __builtin_amdgcn_readfirstlane(__hip_atomic_load((GAS u32*)c, RLX));
      if (v >= need) break;
      if (spins > SPIN_CAP) { __hip_atomic_store((GAS u32*)err, 1u, RLX); break; }
      if ((spins & 63) == 63 && __builtin_amdgcn_readfirstlane(__hip_atomic_load((GAS u32*)err, RLX))) break;
      __builtin_amdgcn_s_sleep(8);
    }
    __builtin_amdgcn_fence(__ATOMIC_ACQUIRE, "agent");
    asm volatile("s_waitcnt vmcnt(0)" ::: "memory");
  }
  __syncthreads();
}
__device__ __forceinline__ void publish_cnt(u64* sy, int ci) {
  asm volatile("s_waitcnt vmcnt(0)" ::: "memory");
  __syncthreads();
  if (threadIdx.x == 0) __hip_atomic_fetch_add((GAS u32*)cnt_ptr(sy, ci), 1u, RLX);
}
__device__ __forceinline__ int dequeue(u64* sy, int ci, char* lds) {
  __syncthreads();
  if (threadIdx.x == 0) *(volatile int*)(lds + L_QW) = (int)__hip_atomic_fetch_add((GAS u32*)cnt_ptr(sy, ci), 1u, RLX);
  __syncthreads();
  return __builtin_amdgcn_readfirstlane(*(volatile int*)(lds + L_QW));
}
__device__ __forceinline__ void wait_gran_slow(u64* sy, u64* g, u32 tag) {
  u32* err = (u32*)(sy + SY_ERR);
  for (int spins = 0;; ++spins) {
    u64 x = __hip_atomic_load((GAS u64*)g, RLX);
    if (__builtin_amdgcn_ballot_w64((u32)(x >> 32) != tag) == 0) break;
    if (spins > SPIN_CAP) { __hip_atomic_store((GAS u32*)err, 1u, RLX); break; }
    if ((spins & 63) == 63 && __builtin_amdgcn_readfirstlane(__hip_atomic_load((GAS u32*)err, RLX))) break;
    __builtin_amdgcn_s_sleep(8);
  }
}

struct TailArgs { const u16* Tp; const u16* Vt; u4v* P12; u2v* P0; u16* out; };
__device__ __forceinline__ void tail_s1(const ChainArgs a, const TailArgs t, char* lds, int s, int hg) {
  const int tid = threadIdx.x, lane = tid & 63, wave = __builtin_amdgcn_readfirstlane(tid >> 6);
  const int l15 = lane & 15, lg = lane >> 4, h = 4 * hg + (wave >> 1), u = wave & 1;
  wait_cnt(a.sy, C_T + (s >> 3), 56);
  if (wave == 7) load_xseg(a.x, s, lds, lane);
  bf8 tf[14];
  {
    const u16* tp = t.Tp + (long)(32 * s + 16 * u + l15) * (NH * DM) + h * DM + 8 * lg;
#pragma unroll
    for (int ks = 0; ks < 14; ++ks) tf[ks] = *(const bf8*)(tp + 32 * ks);
  }
  if (s > 0) {
    if (wave == 0) wait_gran_slow(a.sy, (a.sy + SY_MEMG) + (long)s * 1792 + hg * 64, s + 1);
    __builtin_amdgcn_s_barrier();
  }
  if (tid < 448) {
    if (s == 0) { int r = tid / 56, c8 = tid % 56; *(u4v*)(lds + L_H + hoff(r, c8 * 8)) = *(const u4v*)(a.mem0 + r * DM + c8 * 8); }
    else {
      u64* gm = (a.sy + SY_MEMG) + (long)s * 1792 + tid;
      u64* p[4] = {gm, gm + 448, gm + 896, gm + 1344}; u32 v[4];
      get_gran<4>(p, s + 1, v, (u32*)(a.sy + SY_ERR));
      int hi = tid >= 224 ? 1 : 0, cp = tid - 224 * hi;
#pragma unroll
      for (int k = 0; k < 4; ++k) *(u32*)(lds + L_H + hoff(2 * k + hi, 2 * cp)) = v[k];
    }
  }
  asm volatile("s_waitcnt vmcnt(0)" ::: "memory");
  __syncthreads();
  f4 sv[3];
#pragma unroll
  for (int kt = 0; kt < 3; ++kt) {
    f4 a0 = {0.f, 0.f, 0.f, 0.f}, a1 = {0.f, 0.f, 0.f, 0.f};
#pragma unroll
    for (int ks = 0; ks < 14; ks += 2) {
      a0 = __builtin_amdgcn_mfma_f32_16x16x32_bf16(*(const bf8*)(lds + L_H + hoff(KB3[kt] + l15, 32 * ks + 8 * lg)), tf[ks], a0, 0, 0, 0);
      a1 = __builtin_amdgcn_mfma_f32_16x16x32_bf16(*(const bf8*)(lds + L_H + hoff(KB3[kt] + l15, 32 * ks + 32 + 8 * lg)), tf[ks + 1], a1, 0, 0, 0);
    }
    sv[kt] = (a0 + a1) * 0.0625f;
  }
  const int tq = 16 * u + l15;
  float mx = -INFINITY;
#pragma unroll
  for (int kt = 0; kt < 3; ++kt)
#pragma unroll
    for (int r = 0; r < 4; ++r) {
      bool ok = kt == 0 ? lg < 2 : (16 * (kt - 1) + 4 * lg + r <= tq);
      sv[kt][r] = ok ? sv[kt][r] : -INFINITY; mx = fmaxf(mx, sv[kt][r]);
    }
  mx = swap32_max(swap16_max(mx));
  float sum = 0.f;
#pragma unroll
  for (int kt = 0; kt < 3; ++kt)
#pragma unroll
    for (int r = 0; r < 4; ++r) { float p = __expf(sv[kt][r] - mx); sv[kt][r] = p; sum += p; }
  sum = swap32_add(swap16_add(sum));
  float inv = 1.f / sum;
  s8 p12 = cat8(ck4(sv[1] * inv), ck4(sv[2] * inv)); s4 p0 = ck4(sv[0] * inv);
  long pi = ((long)(s * 16 + h) * 2 + u) * 64 + lane;
  st16_sc1(t.P12 + pi, __builtin_bit_cast(u4v, p12)); st8_sc1(t.P0 + pi, __builtin_bit_cast(u2v, p0));
  publish_cnt(a.sy, C_P + s);
}
__device__ __forceinline__ void tail_s2(const ChainArgs a, const TailArgs t, char* lds, int s, int jq) {
  const int tid = threadIdx.x, lane = tid & 63, wave = __builtin_amdgcn_readfirstlane(tid >> 6);
  const int l15 = lane & 15, lg = lane >> 4;
  wait_cnt(a.sy, C_V + (s >> 3), 56);
  u4v vf[16];
  if (wave < 7) {
    const u16* vp = t.Vt + (long)(112 * jq + 16 * wave + l15) * S_ + 32 * s + 8 * lg;
#pragma unroll
    for (int h = 0; h < 16; ++h) vf[h] = *(const u4v*)(vp + (long)h * DM * S_);
  }
  if (wave == 0) wait_gran_slow(a.sy, a.sy + SY_FLV + s * 64 + (l15 * 4 + jq), s + 1);
  wait_cnt(a.sy, C_P + s, 4);
  if (wave < 7) {
    f4 o1[2], o2[2];
#pragma unroll
    for (int u = 0; u < 2; ++u) { o1[u] = f4{0.f, 0.f, 0.f, 0.f}; o2[u] = f4{0.f, 0.f, 0.f, 0.f}; }
    const u64* vmp = a.vm + ((long)(s * 64 + jq) * 7 + wave) * 32 + (lane & 31);
    const u4v* pp = t.P12 + (long)s * 2048 + lane; const u2v* pq = t.P0 + (long)s * 2048 + lane;
#pragma unroll
    for (int h = 0; h < 16; ++h) {
      u64 vm = vmp[(long)h * 4 * 7 * 32];
      if (lg >= 2) vm = 0;
#pragma unroll
      for (int u = 0; u < 2; ++u) {
        o1[u] = MFMA32(vf[h], pp[(h * 2 + u) * 64], o1[u]);
        o2[u] = MFMA16(__builtin_bit_cast(s4, vm), __builtin_bit_cast(s4, pq[(h * 2 + u) * 64]), o2[u]);
      }
    }
#pragma unroll
    for (int u = 0; u < 2; ++u) {
      f4 o = o1[u] + o2[u];
      *(u2v*)(t.out + (long)(32 * s + 16 * u + l15) * 896 + 112 * jq + 16 * wave + 4 * lg) = u2v{cvt2(o[0], o[1]), cvt2(o[2], o[3])};
    }
  }
}
struct MegaArgs {
  ChainArgs c; TailArgs t;
  const u16* Bx; const u16* ckv; const u16* wq; const u16* wuk; const u16* wuv; const u16* wo;
  u16* Qb; u16* Kb; u16* Vb; u16* Ctx; u16* Tp; u16* Vt;
};
constexpr int MEGA_LDS = 163840;
__global__ __launch_bounds__(512, 2) void mega_kernel(MegaArgs g) {
  extern __shared__ __attribute__((aligned(16))) char lds[];
  if (blockIdx.x < 64) { chain_body(g.c, lds, blockIdx.x); return; }
  u64* sy = g.c.sy;
  for (;;) {
    int it = dequeue(sy, C_Q0 + 0, lds);
    if (it >= 768) break;
    int h = it / 48, r = it % 48, mat = r >> 4, m0 = ((r & 15) >> 1) * 256, n0 = h * 256 + (r & 1) * 128;
    const u16* A = mat == 2 ? g.Bx + (long)m0 * DM : g.ckv + (long)m0 * DC;
    const u16* B = (mat == 0 ? g.wuk : mat == 1 ? g.wuv : g.wq) + n0;
    u16* C = (mat == 0 ? g.Kb : mat == 1 ? g.Vb : g.Qb) + (long)m0 * HD + n0;
    gemm_item<256, true, 0>(lds, A, mat == 2 ? DM : DC, B, HD, mat == 2 ? 7 : 16, C, HD, 128);
    publish_cnt(sy, C_H + h);
  }
  for (;;) {
    int it = dequeue(sy, C_Q0 + 1, lds);
    if (it >= 128) break;
    int qb = 7 - (it >> 4), h = it & 15;
    wait_cnt(sy, C_H + h, 48);
    attn_item(lds, g.Qb, g.Kb, g.Vb, g.Ctx, h, qb);
    publish_cnt(sy, C_C + qb);
  }
  for (;;) {
    int it = dequeue(sy, C_Q0 + 2, lds);
    if (it >= 448) break;
    int rb = it / 56, n0 = (it % 56) * 128;
    gemm_item<256, false, 0>(lds, g.c.x + (long)rb * 256 * DM, DM, g.c.At + (long)n0 * DM, DM, 7, g.Tp + (long)rb * 256 * (NH * DM) + n0, NH * DM, 128);
    publish_cnt(sy, C_T + rb);
  }
  for (;;) {
    int it = dequeue(sy, C_Q0 + 3, lds);
    if (it >= 448) break;
    int rb = it / 56, n0 = (it % 56) * 128;
    gemm_item<256, false, 1>(lds, g.c.x + (long)rb * 256 * DM, DM, g.c.Bs + (long)n0 * DM, DM, 7, g.Vt + (long)n0 * S_ + rb * 256, S_, 128);
    publish_cnt(sy, C_V + rb);
  }
  for (;;) {
    int it = dequeue(sy, C_Q0 + 4, lds);
    if (it >= 64) break;
    int rb = 15 - (it >> 2), n0 = (it & 3) * 128;
    wait_cnt(sy, C_C + (rb >> 1), 16);
    gemm_item<128, true, 2>(lds, g.Ctx + (long)rb * 128 * HD, HD, g.wo + n0, DM, 64, g.t.out + (long)rb * 128 * 896 + 448 + n0, 896, DM - n0);
  }
  for (;;) {
    int it = dequeue(sy, C_Q0 + 5, lds);
    if (it >= 512) break;
    int s = it >> 3, r = it & 7;
    if (r < 4) tail_s1(g.c, g.t, lds, s, r); else tail_s2(g.c, g.t, lds, s, r - 4);
  }
}

static inline GemmArgs mk(const u16* A, const u16* B, u16* C, long sA, long sB, long sC, int lda, int ldb, int ldc, int M, int N, int K) {
  GemmArgs g; g.A = A; g.B = B; g.C = C; g.sA = sA; g.sB = sB; g.sC = sC; g.lda = lda; g.ldb = ldb; g.ldc = ldc; g.M = M; g.N = N; g.K = K; return g;
}
extern "C" void kernel_launch(void* const* d_in, const int* in_sizes, int n_in, void* d_out, int out_size, void* d_ws, size_t ws_size,
                              hipStream_t stream) {
  const u16* Ax = (const u16*)d_in[0]; const u16* mem0 = (const u16*)d_in[1];
  const u16* Wq = (const u16*)d_in[2]; const u16* Wk = (const u16*)d_in[3]; const u16* Wv = (const u16*)d_in[4]; const u16* Wo = (const u16*)d_in[5];
  char* ws = (char*)d_ws;
  size_t off = 0;
  auto alloc = [&](size_t bytes) { char* p = ws + off; off += (bytes + 255) & ~(size_t)255; return p; };
  u64* sy = (u64*)alloc((size_t)SY_END * 8);
  size_t sync_bytes = off;
  const size_t big = (size_t)S_ * HD * 2, big2 = (size_t)S_ * NH * DM * 2;
  u16* At = (u16*)alloc((size_t)NH * DM * DM * 2); u16* Bs = (u16*)alloc((size_t)NH * DM * DM * 2);
  MegaArgs g;
  g.c.x = Ax; g.c.mem0 = mem0; g.c.At = At; g.c.Bs = Bs; g.c.sy = sy; g.c.vm = (u64*)alloc((size_t)64 * 64 * 7 * 32 * 8);
  g.Tp = (u16*)alloc(big2); g.Vt = (u16*)alloc(big2);
  g.t.Tp = g.Tp; g.t.Vt = g.Vt; g.t.P12 = (u4v*)alloc((size_t)64 * 2048 * 16); g.t.P0 = (u2v*)alloc((size_t)64 * 2048 * 8); g.t.out = (u16*)d_out;
  g.Bx = (const u16*)d_in[7]; g.ckv = (const u16*)d_in[8]; g.wq = (const u16*)d_in[9]; g.wuk = (const u16*)d_in[10]; g.wuv = (const u16*)d_in[11]; g.wo = (const u16*)d_in[12];
  g.Qb = (u16*)alloc(big); g.Kb = (u16*)alloc(big); g.Vb = (u16*)alloc(big); g.Ctx = (u16*)alloc(big);

  (void)hipMemsetAsync(ws, 0, sync_bytes, stream);
  gemm_kernel<64, false, false><<<dim3(7, 4, 16), 256, 0, stream>>>(mk(Wk, Wq, At, 256, 256, (long)DM * DM, HD, HD, DM, DM, DM, 256));
  gemm_kernel<64, true, true><<<dim3(7, 4, 16), 256, 0, stream>>>(mk(Wv, Wo, Bs, 256, (long)256 * DM, (long)DM * DM, HD, DM, DM, DM, DM, 256));
  (void)hipFuncSetAttribute((const void*)mega_kernel, hipFuncAttributeMaxDynamicSharedMemorySize, MEGA_LDS);
  mega_kernel<<<256, 512, MEGA_LDS, stream>>>(g);
}
```

```cpp
#include <hip/hip_runtime.h>
#include <hip/hip_bf16.h>
#include <stdint.h>
#include <type_traits>

typedef unsigned short u16;
typedef unsigned int u32;
typedef unsigned long long u64;
typedef __bf16 bf8 __attribute__((ext_vector_type(8)));
typedef short s4 __attribute__((ext_vector_type(4)));
typedef short s8 __attribute__((ext_vector_type(8)));
typedef float f4 __attribute__((ext_vector_type(4)));
typedef float f16v __attribute__((ext_vector_type(16)));
typedef u32 u4v __attribute__((ext_vector_type(4)));
typedef u32 u2v __attribute__((ext_vector_type(2)));
#define LAS __attribute__((address_space(3)))
#define GAS __attribute__((address_space(1)))

static __device__ __forceinline__ u16 f2bf(float f) {
  u32 u = __float_as_uint(f);
  u += 0x7fffu + ((u >> 16) & 1u);
  return (u16)(u >> 16);
}
static __device__ __forceinline__ float bf2f(u16 x) { return __uint_as_float(((u32)x) << 16); }
static __device__ __forceinline__ u32 pack2(float a, float b) { return (u32)f2bf(a) | ((u32)f2bf(b) << 16); }
static __device__ __forceinline__ bf8 as_bf8(u4v v) { return __builtin_bit_cast(bf8, v); }
static __device__ __forceinline__ s4 tr_read(const char* lds_base, unsigned off) {
  return __builtin_amdgcn_ds_read_tr16_b64_v4i16((LAS s4*)(lds_base + off));
}

__device__ __forceinline__ int opq(int v) { asm volatile("" : "+v"(v)); return v; }
constexpr int S_ = 2048, DM = 448, NH = 16, DH = 256, HD = 4096, MEM = 8, SEG = 32, NSEG = 64, DC = 1024;

__device__ __forceinline__ unsigned off_b(unsigned row, unsigned ch) { return 256u * row + 16u * (ch ^ (((row & 3) << 2) | ((row >> 2) & 3))); }
typedef __bf16 bf2 __attribute__((ext_vector_type(2)));
__device__ __forceinline__ u32 cvt2(float a, float b) { bf2 r = {(__bf16)a, (__bf16)b}; return __builtin_bit_cast(u32, r); }
__device__ __forceinline__ u4v cvt8(f4 a, f4 b) { return u4v{cvt2(a[0], a[1]), cvt2(a[2], a[3]), cvt2(b[0], b[1]), cvt2(b[2], b[3])}; }
typedef __amdgpu_buffer_rsrc_t rsrc_t;
__device__ __forceinline__ rsrc_t mk_rsrc(const void* p) { return __builtin_amdgcn_make_buffer_rsrc((void*)p, 0, 0x7fffffff, 0x00020000); }
__device__ __forceinline__ u4v bld(rsrc_t r, unsigned v, unsigned s) { return __builtin_amdgcn_raw_buffer_load_b128(r, v, s, 16); }
__device__ __forceinline__ void bst(u4v d, const void* p, unsigned v, unsigned s) { __builtin_amdgcn_raw_buffer_store_b128(d, mk_rsrc((const char*)p + s), v, 0, 16); }
#define SB() __builtin_amdgcn_sched_barrier(0)
constexpr int L_TK = 163840 - 32;
__device__ __forceinline__ u32 tk_draw(u32* q, int t0 = 0) {
  u32 t = 0;
  if (opq(threadIdx.x) == t0) t = __hip_atomic_fetch_add((GAS u32*)q, 1u, __ATOMIC_RELAXED, __HIP_MEMORY_SCOPE_AGENT);
  return t;
}
__device__ __forceinline__ void tk_post(char* lds, u32 t, int t0 = 0) { if (opq(threadIdx.x) == t0) *(volatile LAS u32*)(lds + L_TK) = t; }
__device__ __forceinline__ int tk_read(char* lds) { return __builtin_amdgcn_readfirstlane(*(volatile LAS int*)(lds + L_TK)); }
#define LBAR() asm volatile("s_waitcnt lgkmcnt(0)\n\ts_barrier" ::: "memory")
__device__ __forceinline__ void st16_sc1(void* p, u4v v) { asm volatile("global_store_dwordx4 %0, %1, off sc1\n\ts_nop 2" :: "v"(p), "v"(v) : "memory"); }
__device__ __forceinline__ void p5_item(char* lds, const u16* __restrict__ A, const u16* __restrict__ B, u16* __restrict__ C, char* pp, int kq, u32* cnt, u32* nq) {
  constexpr int SA = 16384, STG = 49152, PS = 14 * 8192, nk = 16;
  const int tid = opq(threadIdx.x), lane = tid & 63, wave = __builtin_amdgcn_readfirstlane(tid >> 6);
  const int wm = wave >> 1, wn = wave & 1, l15 = lane & 15, lg = lane >> 4;
  unsigned ao, bo, bo2;
  { int row = wave * 16 + (lane >> 3); ao = (unsigned)(row * DH + (((lane & 7) ^ (row & 7)) << 3)) * 2u; }
  {
    int r0 = wave * 8 + (lane >> 4), r1 = r0 + 4;
    int c0 = (lane & 15) ^ (((r0 & 3) << 2) | ((r0 >> 2) & 3)), c1 = (lane & 15) ^ (((r1 & 3) << 2) | ((r1 >> 2) & 3));
    c0 = c0 < 14 ? c0 : c0 - 4; c1 = c1 < 14 ? c1 : c1 - 4;
    r0 = (r0 & ~31) + 16 * ((r0 >> 2) & 1) + 4 * ((r0 >> 3) & 3) + (r0 & 3); r1 = (r1 & ~31) + 16 * ((r1 >> 2) & 1) + 4 * ((r1 >> 3) & 3) + (r1 & 3);
    bo = (unsigned)(r0 * DM + c0 * 8) * 2u; bo2 = (unsigned)(r1 * DM + c1 * 8) * 2u;
  }
  auto issue = [&](int kt, int buf) {
    char* sa = lds + buf * STG + wave * 2048; char* sb = sa + SA;
    const char* ga = (const char*)A + (long)(kt >> 2) * (S_ * DH * 2) + (kt & 3) * 128 + ao;
    const char* gb = (const char*)B + (long)kt * 128 * DM;
    __builtin_amdgcn_global_load_lds((const GAS void*)ga, (LAS void*)sa, 16, 0, 0);
    __builtin_amdgcn_global_load_lds((const GAS void*)(ga + 16 * DH), (LAS void*)(sa + 1024), 16, 0, 0);
#pragma unroll
    for (int u = 0; u < 2; ++u) {
      __builtin_amdgcn_global_load_lds((const GAS void*)(gb + bo + u * 224), (LAS void*)(sb + u * 16384), 16, 0, 0);
      __builtin_amdgcn_global_load_lds((const GAS void*)(gb + bo2 + u * 224), (LAS void*)(sb + u * 16384 + 1024), 16, 0, 0);
    }
  };
  f4 acc[2][7];
#pragma unroll
  for (int i = 0; i < 2; ++i)
#pragma unroll
    for (int j = 0; j < 7; ++j) acc[i][j] = f4{0.f, 0.f, 0.f, 0.f};
  issue(0, 0);
  issue(1, 1);
  int buf = 0;
  for (int kt = 0; kt < nk; ++kt) {
    if (kt + 1 < nk) asm volatile("s_waitcnt vmcnt(6)" ::: "memory");
    else asm volatile("s_waitcnt vmcnt(0)" ::: "memory");
    __builtin_amdgcn_s_barrier();
    asm volatile("" ::: "memory");
    if (kt + 2 < nk) issue(kt + 2, buf == 0 ? 2 : buf - 1);
    const char* sa = lds + buf * STG; const char* sb = sa + SA + wn * 16384;
#pragma unroll
    for (int ks = 0; ks < 2; ++ks) {
      bf8 af[2], bfr[7];
#pragma unroll
      for (int i = 0; i < 2; ++i) {
        int row = wm * 32 + i * 16 + l15, ch = ks * 4 + lg;
        af[i] = *(const bf8*)(sa + row * 128 + ((ch ^ (row & 7)) << 4));
      }
#pragma unroll
      for (int j = 0; j < 7; ++j) {
        unsigned q = l15 >> 2, p = lane & 3;
        s4 lo = tr_read(sb, off_b(32 * ks + 8 * lg + q, 2 * j + (p >> 1)) + 8 * (p & 1));
        s4 hi = tr_read(sb, off_b(32 * ks + 8 * lg + 4 + q, 2 * j + (p >> 1)) + 8 * (p & 1));
        s8 v = {lo[0], lo[1], lo[2], lo[3], hi[0], hi[1], hi[2], hi[3]};
        bfr[j] = __builtin_bit_cast(bf8, v);
      }
#pragma unroll
      for (int j = 0; j < 7; ++j)
#pragma unroll
        for (int i = 0; i < 2; ++i) acc[i][j] = __builtin_amdgcn_mfma_f32_16x16x32_bf16(bfr[j], af[i], acc[i][j], 0, 0, 0);
    }
    buf = buf == 2 ? 0 : buf + 1;
  }
  {
    const unsigned po = tid * 16; const u32 tk = tk_draw(nq);
#pragma unroll
    for (int e = 0; e < 14; ++e) bst(__builtin_bit_cast(u4v, acc[e / 7][e % 7]), pp, po, kq * PS + e * 8192);
    asm volatile("s_waitcnt vmcnt(0)" ::: "memory");
    tk_post(lds, tk);
    __syncthreads();
    if (tid == 0) *(volatile int*)lds = (int)__hip_atomic_fetch_add((GAS u32*)cnt, 1u, __ATOMIC_RELAXED, __HIP_MEMORY_SCOPE_AGENT);
    __syncthreads();
    if (__builtin_amdgcn_readfirstlane(*(volatile int*)lds) != 3) return;
    const rsrc_t rp = mk_rsrc(pp);
    u4v pv[3][14];
#pragma unroll
    for (int e = 0; e < 14; ++e)
#pragma unroll
      for (int k = 0; k < 3; ++k) pv[k][e] = bld(rp, po, (k + (k >= kq)) * PS + e * 8192);
#pragma unroll
    for (int e = 0; e < 14; ++e) {
      const f4 a = acc[e / 7][e % 7], b = __builtin_bit_cast(f4, pv[0][e]), c = __builtin_bit_cast(f4, pv[1][e]), d = __builtin_bit_cast(f4, pv[2][e]);
      acc[e / 7][e % 7] = kq < 2 ? (a + b) + (c + d) : (b + c) + (a + d);
    }
  }
  __syncthreads();
  constexpr int P = 448 + 16;
#pragma unroll
  for (int i = 0; i < 2; ++i)
#pragma unroll
    for (int j = 0; j < 7; ++j) {
      int m = wm * 32 + i * 16 + l15, n = wn * 112 + j * 16 + 4 * lg;
      *(u2v*)(lds + m * P + n * 2) = u2v{cvt2(acc[i][j][0], acc[i][j][1]), cvt2(acc[i][j][2], acc[i][j][3])};
    }
  __syncthreads();
#pragma unroll
  for (int it = 0; it < 7; ++it) {
    int id = it * 512 + tid, m = id / 28, ch = id % 28;
    *(u4v*)(C + (long)m * 896 + ch * 8) = *(const u4v*)(lds + m * P + ch * 16);
  }
}
struct ChainArgs {
  const u16* x; const u16* mem0;
  u64* sy;
  u64* vm;
  const char* VU;
  char* AtU; char* BsU;
  u64* pf;
};
constexpr int PFX = 16, PF1 = 32, PF2 = PF1 + 256, PN1 = PF2 + 128, PN2 = PN1 + 448, PF_END = PN2 + 128;
constexpr int L_PC = 163840 - 64;
constexpr int SY_MEMG = 0, SY_ACC = 64 * 1792, SY_NRM = SY_ACC + 63 * 3584, SY_ERR = SY_NRM + 64, SY_FLV = SY_ERR + 512 + 64 * 384, SY_CNT = SY_FLV + 64 * 64, SY_END = SY_CNT + 192 * 32;
#define RLX __ATOMIC_RELAXED, __HIP_MEMORY_SCOPE_AGENT
enum { C_Q0 = 0, C_H = 8, C_T = 24, C_V = 32, C_P = 40, C_X = 120, C_C = 128 };
__device__ __forceinline__ u32* cnt_ptr(u64* sy, int i) { return (u32*)(sy + SY_CNT + i * 32); }
constexpr int SPIN_CAP = 1 << 20;
__device__ __forceinline__ u32 peek(const u32* p) {
  u32 v = 0;
  if ((opq(threadIdx.x) & 63) == 0) v = __hip_atomic_load((GAS u32*)p, RLX);
  return __builtin_amdgcn_readfirstlane(v);
}
__device__ __forceinline__ void put_gran(u64* p, u32 tag, u32 val) {
  __hip_atomic_store((GAS u64*)p, ((u64)tag << 32) | val, RLX);
}
template <int N>
__device__ __forceinline__ void get_gran(u64* const (&p)[N], u32 tag, u32 (&v)[N], u32* err) {
  for (int spins = 0;; ++spins) {
    bool ok = true;
#pragma unroll
    for (int k = 0; k < N; ++k) { u64 x = __hip_atomic_load((GAS u64*)p[k], RLX); v[k] = (u32)x; ok &= ((u32)(x >> 32) == tag); }
    if (ok) break;
    if (spins > SPIN_CAP) { __hip_atomic_store((GAS u32*)err, 1u, RLX); break; }
    if ((spins & 255) == 255 && __hip_atomic_load((GAS u32*)err, RLX)) break;
  }
}
__device__ __forceinline__ s8 cat8(s4 a, s4 b) { s8 v = {a[0], a[1], a[2], a[3], b[0], b[1], b[2], b[3]}; return v; }
__device__ __forceinline__ s4 pk4(f4 a) { u2v t = {pack2(a[0], a[1]), pack2(a[2], a[3])}; return __builtin_bit_cast(s4, t); }
#define MFMA32(a, b, c) __builtin_amdgcn_mfma_f32_16x16x32_bf16(__builtin_bit_cast(bf8, a), __builtin_bit_cast(bf8, b), c, 0, 0, 0)
#define MFMA16(a, b, c) __builtin_amdgcn_mfma_f32_16x16x16bf16_1k(a, b, c, 0, 0, 0)
__device__ __forceinline__ unsigned hoff(unsigned row, unsigned c) {
  unsigned ch = c >> 3;
  return row * 896u + (row >> 3) * 128u + (((ch & ~7u) | ((ch & 7u) ^ (row & 7u))) << 4) + (c & 7u) * 2u;
}
constexpr int KB2[3] = {0, 16, 24};
constexpr int KB3[3] = {0, 8, 24};
constexpr int L_B = 0;
constexpr int L_H = 100352;
constexpr int L_S = L_H + 36352;
constexpr int L_W = L_S + 10240;
constexpr int L_V = L_W + 8192;
constexpr int L_N = L_V + 7168;
constexpr int L_P = L_N + 512;
constexpr int L_END = L_P + 640;
static_assert(L_END + 256 <= 163840, "LDS");
typedef __bf16 bf16v __attribute__((ext_vector_type(16)));
typedef u32 u8v __attribute__((ext_vector_type(8)));
#define SMF(a, b, c) __builtin_amdgcn_smfmac_f32_16x16x64_bf16(a, b, c, sidx, 0, 0)
#define DLY2 8
#define DLY3 3
#define DLY4 0
#define W3B 4
#define STAMP(k)
#define STFLUSH()
#define CST(k)

__device__ __forceinline__ void load_xseg(const u16* x, int seg, char* lds, int lane) {
  lane = opq(lane);
  int lb = L_H + 7296; asm volatile("" : "+s"(lb));
  const char* xb = (const char*)(x + (long)32 * seg * DM);
  if (lane < 56) {
#pragma unroll
    for (int row = 0; row < 32; ++row) {
      const char* src = (xb + row * 896) + (unsigned)((lane ^ (row & 7)) << 4);
      __builtin_amdgcn_global_load_lds((const GAS void*)src, (LAS void*)(lds + lb + row * 896 + (row >> 3) * 128), 16, 0, 0);
    }
  }
}

__device__ __forceinline__ s4 ck4(f4 a) { u2v t = {cvt2(a[0], a[1]), cvt2(a[2], a[3])}; return __builtin_bit_cast(s4, t); }
template <int CTRL> __device__ __forceinline__ float dppf(float v) {
  return __int_as_float(__builtin_amdgcn_update_dpp(0, __float_as_int(v), CTRL, 0xf, 0xf, true));
}
__device__ __forceinline__ float swap16_max(float v) { auto r = __builtin_amdgcn_permlane16_swap(__float_as_uint(v), __float_as_uint(v), false, false); return fmaxf(__uint_as_float(r[0]), __uint_as_float(r[1])); }
__device__ __forceinline__ float swap32_max(float v) { auto r = __builtin_amdgcn_permlane32_swap(__float_as_uint(v), __float_as_uint(v), false, false); return fmaxf(__uint_as_float(r[0]), __uint_as_float(r[1])); }
__device__ __forceinline__ float swap16_add(float v) { auto r = __builtin_amdgcn_permlane16_swap(__float_as_uint(v), __float_as_uint(v), false, false); return __uint_as_float(r[0]) + __uint_as_float(r[1]); }
__device__ __forceinline__ float swap32_add(float v) { auto r = __builtin_amdgcn_permlane32_swap(__float_as_uint(v), __float_as_uint(v), false, false); return __uint_as_float(r[0]) + __uint_as_float(r[1]); }

__device__ __forceinline__ bf16v gl_unit(rsrc_t r, unsigned vo, unsigned so) {
  u4v a = bld(r, vo, so), b = bld(r, vo + 1024, so);
  u8v v = {a[0], a[1], a[2], a[3], b[0], b[1], b[2], b[3]};
  return __builtin_bit_cast(bf16v, v);
}
__device__ __forceinline__ bf16v lds_unit(const char* p0, const char* p1) {
  u4v a = *(const u4v*)p0, b = *(const u4v*)p1;
  u8v v = {a[0], a[1], a[2], a[3], b[0], b[1], b[2], b[3]};
  return __builtin_bit_cast(bf16v, v);
}
__device__ __forceinline__ void load_xgrp(const u16* x, int seg, int g, char* lds) {
  const int lane = opq(threadIdx.x) & 63;
  const char* xb = (const char*)(x + (long)(32 * seg + 8 * g) * DM);
  char* lb = lds + L_H + 7296 * (g + 1);
  if (lane < 56) {
#pragma unroll
    for (int row = 0; row < 8; ++row)
      __builtin_amdgcn_global_load_lds((const GAS void*)(xb + row * 896 + (unsigned)((lane ^ row) << 4)), (LAS void*)(lb + row * 896), 16, 0, 0);
  }
}
__device__ __forceinline__ void wait_vblk(const ChainArgs& a, int blk) {
  u32* c = cnt_ptr(a.sy, C_V + blk); u32* err = (u32*)(a.sy + SY_ERR);
  for (int spins = 0;; ++spins) {
    if (peek(c) >= 224) break;
    if (spins > SPIN_CAP) { __hip_atomic_store((GAS u32*)err, 1u, RLX); break; }
    if ((spins & 63) == 63 && peek(err)) break;
    __builtin_amdgcn_s_sleep(4);
  }
  asm volatile("" ::: "memory");
}
__device__ __forceinline__ void load_vseg(const ChainArgs& a, int seg, char* lds, int wg) {
  const int lane = opq(threadIdx.x) & 63;
  const char* vp = a.VU + (long)(seg * 448 + wg * 7) * 1024 + lane * 16;
#pragma unroll
  for (int w = 0; w < 7; ++w)
    __builtin_amdgcn_global_load_lds((const GAS void*)(vp + w * 1024), (LAS void*)(lds + L_V + w * 1024), 16, 0, 16);
}

__device__ __forceinline__ float wave_max(float v) {
  v = fmaxf(v, dppf<0xB1>(v)); v = fmaxf(v, dppf<0x4E>(v)); v = fmaxf(v, dppf<0x124>(v)); v = fmaxf(v, dppf<0x128>(v));
  return swap32_max(swap16_max(v));
}
__device__ __forceinline__ float absmax4(f4 a) { return fmaxf(fmaxf(fabsf(a[0]), fabsf(a[1])), fmaxf(fabsf(a[2]), fabsf(a[3]))); }
typedef float f2 __attribute__((ext_vector_type(2)));
__device__ __forceinline__ u32 dec2(u32 lo, u32 hi, float inv8) {
  f2 v = {(float)(int)hi, (float)(int)__builtin_amdgcn_alignbit(hi, lo, 3)};
  v *= inv8;
  return cvt2(v[0], v[1]);
}
template <int DLY, bool VLD>
__device__ __forceinline__ void gather_mem(const ChainArgs& a, int s, char* lds, float inv8, int wg, int wave) {
  const int tid = opq(threadIdx.x);
  const char* g = (const char*)(a.sy + SY_ACC + (long)(s - 1) * 3584);
  unsigned go = (tid >> 2) * 128 + (tid & 3) * 16;
  const unsigned c = 16 * (tid >> 5) + 4 * ((tid >> 3) & 3), i = tid & 7;
  LAS char* w0 = (LAS char*)lds + L_H + hoff(i, c); LAS char* w1 = (LAS char*)lds + L_H + hoff(i, c + 224);
  unsigned vo = (tid & 63) * 16;
  const char* vp = a.VU + (long)(s * 448 + wg * 7 + wave) * 1024;
  asm volatile("" : "+v"(go), "+v"(w0), "+v"(w1), "+v"(vo), "+s"(vp));
  if (DLY) __builtin_amdgcn_s_sleep(DLY);
  u4v v0, v1;
  asm volatile("global_load_dwordx4 %0, %2, %3 sc1\n\tglobal_load_dwordx4 %1, %2, %4 sc1" : "=&v"(v0), "=&v"(v1) : "v"(go), "s"(g), "s"(g + 14336) : "memory");
  if (VLD) {
    __builtin_amdgcn_global_load_lds((const GAS void*)(vp + vo), (LAS void*)(lds + L_V + wave * 1024), 16, 0, 16);
    asm volatile("s_waitcnt vmcnt(1)" : "+v"(v0), "+v"(v1) :: "memory");
  } else asm volatile("s_waitcnt vmcnt(0)" : "+v"(v0), "+v"(v1) :: "memory");
  for (int spins = 0;; ++spins) {
    if ((v0[0] & v0[2] & v1[0] & v1[2]) & 16u) break;
    if (spins > SPIN_CAP) { __hip_atomic_store((GAS u32*)(a.sy + SY_ERR), 1u, RLX); break; }
    if ((spins & 255) == 255 && __hip_atomic_load((GAS u32*)(a.sy + SY_ERR), RLX)) break;
    asm volatile("global_load_dwordx4 %0, %2, %3 sc1\n\tglobal_load_dwordx4 %1, %2, %4 sc1\n\ts_waitcnt vmcnt(0)" : "=&v"(v0), "=&v"(v1) : "v"(go), "s"(g), "s"(g + 14336) : "memory");
  }
  *(LAS u2v*)w0 = u2v{dec2(v0[0], v0[1], inv8), dec2(v0[2], v0[3], inv8)};
  *(LAS u2v*)w1 = u2v{dec2(v1[0], v1[1], inv8), dec2(v1[2], v1[3], inv8)};
}
__device__ __forceinline__ void row_max2(float& m0, float& m1) {
  asm volatile(
      "s_nop 1\n\tv_permlane32_swap_b32 %0, %1\n\tv_max_f32 %0, %0, %1\n\tv_mov_b32 %1, %0\n\ts_nop 1\n\t"
      "v_permlane16_swap_b32 %0, %1\n\tv_max_f32 %0, %0, %1\n\ts_nop 1\n\t"
      "v_max_f32_dpp %0, %0, %0 quad_perm:[1,0,3,2] row_mask:0xf bank_mask:0xf\n\ts_nop 1\n\t"
      "v_max_f32_dpp %0, %0, %0 quad_perm:[2,3,0,1] row_mask:0xf bank_mask:0xf\n\ts_nop 1\n\t"
      "v_max_f32_dpp %0, %0, %0 row_half_mirror row_mask:0xf bank_mask:0xf\n\ts_nop 1\n\t"
      "v_max_f32_dpp %0, %0, %0 row_mirror row_mask:0xf bank_mask:0xf\n\tv_mov_b32 %1, %0\n\ts_nop 1\n\t"
      "v_permlane32_swap_b32 %0, %1\n\ts_nop 0"
      : "+v"(m0), "+v"(m1));
}
#define BAR0() asm volatile("s_waitcnt vmcnt(0)\n\ts_barrier" ::: "memory")
#define BAR() asm volatile("s_waitcnt lgkmcnt(0)\n\ts_barrier" ::: "memory")

#define JB 3
#define PSL 6
__device__ __forceinline__ void grid_wait(const u64* pf, u64 tag) {
  const rsrc_t r = mk_rsrc(pf + PF1);
  const unsigned v = (opq(threadIdx.x) & 63) * 16;
  const u32 lo = (u32)tag, hi = (u32)(tag >> 32);
  for (int spins = 0; spins < SPIN_CAP; ++spins) {
    const u4v a = bld(r, v, 0), b = bld(r, v, 1024);
    const u32 d = (a[0] ^ lo) | (a[1] ^ hi) | (a[2] ^ lo) | (a[3] ^ hi) | (b[0] ^ lo) | (b[1] ^ hi) | (b[2] ^ lo) | (b[3] ^ hi);
    if (__builtin_amdgcn_ballot_w64(d != 0) == 0) break;
    __builtin_amdgcn_s_sleep(4);
    asm volatile("" ::: "memory");
  }
  asm volatile("" ::: "memory");
}

__device__ __forceinline__ void chain_pre(const ChainArgs& a, char* lds) {
  const int lane = opq(threadIdx.x) & 63;
  load_xseg(a.x, 0, lds, lane);
  u4v mv[7];
#pragma unroll
  for (int k = 0; k < 7; ++k) mv[k] = *(const u4v*)(a.mem0 + (lane >> 3) * DM + (8 * k + (lane & 7)) * 8);
#pragma unroll
  for (int k = 0; k < 7; ++k) *(u4v*)(lds + L_H + hoff(lane >> 3, (8 * k + (lane & 7)) * 8)) = mv[k];
}
__device__ __forceinline__ void wait_flags(const u64* f, u64 tag, bool on) {
  for (int spins = 0; spins < SPIN_CAP; ++spins) {
    u64 x = tag;
    if (on) x = __hip_atomic_load((GAS u64*)f, RLX);
    if (__builtin_amdgcn_ballot_w64(x != tag) == 0) break;
    __builtin_amdgcn_s_sleep(PSL);
  }
  asm volatile("" ::: "memory");
}
template <int ROLE>
__device__ __forceinline__ void chain_role(const ChainArgs& a, char* lds, int wg, int wave) {
  constexpr bool LO = ROLE < 2;
  constexpr int rot = LO ? 0 : 4;
  const int tid = opq(threadIdx.x), lane = tid & 63;
  const int h = wg >> 2, jq = wg & 3;
  const u32 sidx = (lane & 15) < 8 ? 0x44444444u : 0xEEEEEEEEu;
  const u64 tag = *(volatile LAS u64*)(lds + L_PC);
  const u64* fl = a.pf + PF1 + (h >> 2) * 32 + (h & 3) * 7 + lane;
  if (ROLE == 3) {
    chain_pre(a, lds);
    wait_flags(fl, tag, lane < 7);
  }
  asm volatile("s_barrier" ::: "memory");
  CST(2);
  bf16v wf[3][7], wh[3];
  {
    const rsrc_t ra = mk_rsrc(a.AtU + (long)h * 28 * 14336);
    const unsigned au = lane * 16, av = (lane ^ 4) * 16;
#pragma unroll
    for (int j = 0; j < 7; ++j) {
      if (j == (ROLE == 3 ? 0 : JB)) {
        SB();
        if (ROLE == 3) wait_flags(fl + 128, tag, lane < 7);
        asm volatile("s_barrier" ::: "memory");
        if (ROLE != 3) {
          const char* bu = a.BsU + (long)(wg * 7 + wave) * 14336 + lane * 16;
#pragma unroll
          for (int i = 0; i < 14; ++i)
            __builtin_amdgcn_global_load_lds((const GAS void*)(bu + i * 1024), (LAS void*)(lds + L_B + wave * 14336 + i * 1024), 16, 0, 16);
        }
        SB();
      }
      const int jc = (j + rot) % 7;
#pragma unroll
      for (int t = 0; t < 3; ++t) wf[t][j] = gl_unit(ra, t == 2 ? av : au, (3 * wave + t) * 14336 + jc * 2048);
      if (j < 3) wh[j] = gl_unit(ra, av, (24 + (wave & 3)) * 14336 + jc * 2048);
    }
    if (LO) {
      const char* aw = a.AtU + (long)(h * 28 + 24 + wave) * 14336 + 3 * 2048 + av;
      __builtin_amdgcn_global_load_lds((const GAS void*)aw, (LAS void*)(lds + L_W + wave * 2048), 16, 0, 16);
      __builtin_amdgcn_global_load_lds((const GAS void*)(aw + 1024), (LAS void*)(lds + L_W + wave * 2048 + 1024), 16, 0, 16);
    }
  }
  SB();
  CST(3);
  if (ROLE == 2 && wave == 6) {
    wait_flags(a.pf + PF2 + h * 8 + lane, tag, lane < 7);
    load_vseg(a, 0, lds, wg);
  }
  if (ROLE == 3) {
    grid_wait(a.pf, tag);
    if (wg == 0 && lane == 0) __hip_atomic_store((GAS u64*)a.pf, tag, RLX);
    CST(11);
    const rsrc_t rn = mk_rsrc(a.pf);
    const unsigned o1 = PN1 * 8 + lane * 32, o2 = PN1 * 8 + (lane < 48 ? lane + 64 : lane) * 32, o3 = PN2 * 8 + (lane >> 1) * 32;
    const f4 b0 = __builtin_bit_cast(f4, bld(rn, o1, 0)), b1 = __builtin_bit_cast(f4, bld(rn, o1, 16)), b2 = __builtin_bit_cast(f4, bld(rn, o2, 0)), b3 = __builtin_bit_cast(f4, bld(rn, o2, 16));
    const f4 x0 = __builtin_bit_cast(f4, bld(rn, o3, 0)), x1 = __builtin_bit_cast(f4, bld(rn, o3, 16));
    const float bn = 4.f * wave_max(fmaxf((b0[0] + b0[1]) + (b0[2] + b0[3]) + (b1[0] + b1[1]) + b1[2], (b2[0] + b2[1]) + (b2[2] + b2[3]) + (b3[0] + b3[1]) + b3[2]));
    const f4 xs = x0 + x1;
    ((float*)(lds + L_N))[lane] = bn * ((lane & 1) ? fmaxf(xs[2], xs[3]) : fmaxf(xs[0], xs[1]));
    if (lane == 0) ((float*)(lds + L_N + 256))[8] = 2.f * bn;
    CST(5);
  }
  SB();
  if (!LO) __builtin_amdgcn_s_setprio(1);
  float inv8 = 0.f;
  const f4 z4 = {0.f, 0.f, 0.f, 0.f};
  auto step = [&](auto LAST, int s) {
    constexpr bool last = decltype(LAST)::value;
    LAS char* ma; LAS char* sp; LAS char* wp; LAS char* ra; unsigned wa, aA, aB, aA2, aB2; u4v sel; f4 acc[4];
    {
      const int lane = opq(threadIdx.x) & 63, l15 = lane & 15, lg = lane >> 4;
      ma = (LAS char*)lds + L_H + (lane & 7) * 896 + (((2 * lg + ((lane >> 3) & 1)) ^ (lane & 7)) << 4);
      asm volatile("" : "+v"(ma));
      if (!last) {
        const unsigned chA = (lg & 1) ? 6 * wave + 4 : 6 * wave, chB = (lg & 1) ? 48 + 2 * (wave & 3) : 6 * wave + 2;
        const unsigned rb = (unsigned)(size_t)(LAS char*)lds + L_H + l15 * 896 + (l15 >> 3) * 128 + 8 * ((lg >> 1) ^ (lg & 1));
        aA = rb + ((chA ^ (l15 & 7)) << 4); aB = rb + ((chB ^ (l15 & 7)) << 4); aA2 = aA ^ 16; aB2 = aB ^ 16;
        const u32 one = ((lg ^ (l15 >> 2)) & 1) ? 0u : (0x3F80u << (16 * (l15 & 1)));
        const int qs = 2 * (l15 >> 3) + ((l15 >> 1) & 1);
        sel = u4v{qs == 0 ? one : 0u, qs == 1 ? one : 0u, qs == 2 ? one : 0u, qs == 3 ? one : 0u};
        sp = (LAS char*)lds + L_S + wave * 1280 + lg * 320 + l15 * 8;
        wp = (LAS char*)lds + L_W + wave * 2048 + lane * 16;
#pragma unroll
        for (int t = 0; t < 4; ++t) acc[t] = z4;
        asm volatile("" : "+v"(aA), "+v"(aB), "+v"(aA2), "+v"(aB2), "+v"(sel), "+v"(sp), "+v"(acc[0]), "+v"(acc[1]), "+v"(acc[2]), "+v"(acc[3]));
        if (LO) asm volatile("" : "+v"(wp));
        else {
          const int q = wave - 4, key = lane < 40 ? lane : lane - 24, tk = key - 8;
          ra = (LAS char*)lds + L_S + q * 320 + key * 8;
          wa = (unsigned)(size_t)(LAS char*)lds + L_P + (4 * (q & 1) + 2 * (q >> 1)) * 80 + (key < 8 ? 64 + 2 * key : 16 * ((tk >> 2) & 3) + 8 * (tk >> 4) + 2 * (tk & 3));
          asm volatile("" : "+v"(ra), "+v"(wa));
        }
      }
    }
    if (ROLE != 3 && s > 0) gather_mem<LO ? DLY2 : DLY3, !last>(a, s, lds, inv8, wg, wave);
    STAMP(0);
    if (ROLE == 3 || s == 0) asm volatile("s_waitcnt vmcnt(0)" ::: "memory");
    if (LO) __builtin_amdgcn_s_setprio(3);
    BAR();
    if (s == 0) CST(4);
    STAMP(1);
#define MF(j) (*(LAS const bf8*)(ma + (((j) + rot) % 7) * 128))
    if (!last) {
      bf8 mf[8]; bf16v bw, hb[3];
      auto hread = [&](auto KT) {
        constexpr int kt = decltype(KT)::value;
        u2v x0, y0, x1, y1;
        asm volatile("ds_read_b64 %0, %1 offset:%2" : "=v"(x0) : "v"(aA), "n"(KB3[kt] * 912) : "memory");
        asm volatile("ds_read_b64 %0, %1 offset:%2" : "=v"(y0) : "v"(aB), "n"(KB3[kt] * 912) : "memory");
        asm volatile("ds_read_b64 %0, %1 offset:%2" : "=v"(x1) : "v"(aA2), "n"(KB3[kt] * 912) : "memory");
        asm volatile("ds_read_b64 %0, %1 offset:%2" : "=v"(y1) : "v"(aB2), "n"(KB3[kt] * 912) : "memory");
        u8v v = {x0[0], x0[1], y0[0], y0[1], x1[0], x1[1], y1[0], y1[1]};
        hb[kt] = __builtin_bit_cast(bf16v, v);
      };
      mf[0] = MF(0); mf[1] = MF(1); mf[2] = MF(2);
      __builtin_amdgcn_sched_barrier(0);
#pragma unroll
      for (int j = 0; j < 7; ++j) {
        if (j == 5) asm volatile("" : "+v"(mf[6]));
#pragma unroll
        for (int t = 0; t < 3; ++t) acc[t] = SMF(mf[j], wf[t][j], acc[t]);
        if (j < 3) acc[3] = SMF(mf[j], wh[j], acc[3]);
        if (LO && j == 3) acc[3] = SMF(mf[3], bw, acc[3]);
        __builtin_amdgcn_sched_barrier(0);
        if (j + 3 < 7) mf[j + 3] = MF(j + 3);
        if (LO && j == 0) { u4v b0 = *(LAS const u4v*)wp, b1 = *(LAS const u4v*)(wp + 1024); bw = __builtin_bit_cast(bf16v, u8v{b0[0], b0[1], b0[2], b0[3], b1[0], b1[1], b1[2], b1[3]}); }
        if (j == 5) { hread(std::integral_constant<int, 1>{}); hread(std::integral_constant<int, 2>{}); }
        __builtin_amdgcn_sched_barrier(0);
      }
      hread(std::integral_constant<int, 0>{});
      __builtin_amdgcn_sched_barrier(0);
      const s8 t23 = cat8(ck4(acc[2]), ck4(acc[3])), t01 = cat8(ck4(acc[0]), ck4(acc[1]));
      f4 d0 = MFMA32(t01, sel, z4), d1 = MFMA32(t23, sel, z4);
      __builtin_amdgcn_sched_barrier(0);
      f4 sa[3] = {z4, z4, z4};
      asm volatile("" : "+v"(sa[0]), "+v"(sa[1]), "+v"(sa[2]));
      __builtin_amdgcn_sched_barrier(0);
      const int e0 = cvt2(d0[0], d0[1]), e1 = cvt2(d0[2], d0[3]), e2 = cvt2(d1[0], d1[1]), e3 = cvt2(d1[2], d1[3]);
      const u4v tw = {(u32)__builtin_amdgcn_update_dpp(e0, e1, 0x128, 0xf, 0xc, false), (u32)__builtin_amdgcn_update_dpp(e1, e0, 0x128, 0xf, 0x3, false),
                      (u32)__builtin_amdgcn_update_dpp(e2, e3, 0x128, 0xf, 0xc, false), (u32)__builtin_amdgcn_update_dpp(e3, e2, 0x128, 0xf, 0x3, false)};
      bf8 ta = as_bf8(tw);
      asm volatile("s_waitcnt lgkmcnt(0)" : "+v"(hb[0]), "+v"(hb[1]), "+v"(hb[2]), "+v"(ta) :: "memory");
#pragma unroll
      for (int kt = 0; kt < 3; ++kt) sa[kt] = SMF(ta, hb[kt], sa[kt]);
      __builtin_amdgcn_sched_barrier(0);
#pragma unroll
      for (int kt = 0; kt < 3; ++kt) {
        auto r0 = __builtin_amdgcn_permlane32_swap(__float_as_uint(sa[kt][0]), __float_as_uint(sa[kt][2]), false, false);
        auto r1 = __builtin_amdgcn_permlane32_swap(__float_as_uint(sa[kt][1]), __float_as_uint(sa[kt][3]), false, false);
        u2v o2 = {__float_as_uint(__uint_as_float(r0[0]) + __uint_as_float(r0[1])), __float_as_uint(__uint_as_float(r1[0]) + __uint_as_float(r1[1]))};
        *(LAS u2v*)(sp + KB3[kt] * 8) = o2;
      }
    }
    STAMP(2);
    if (LO) {
      __builtin_amdgcn_s_setprio(0);
      __builtin_amdgcn_sched_barrier(0);
      const int lane = opq(threadIdx.x) & 63, l15 = lane & 15, lg = lane >> 4;
      const char* ba = lds + L_B + wave * 28672 + lane * 16;
#define BU(j) lds_unit(ba + (j) * 2048, ba + (j) * 2048 + 1024)
      bf8 mf[7]; bf16v bu[14]; f4 va[2];
      if (ROLE == 0) {
#define BP(p) BU(((p) & 1) * 7 + ((p) >> 1))
        mf[0] = MF(0); bu[0] = BP(0); bu[1] = BP(1); mf[1] = MF(1); bu[2] = BP(2);
        __builtin_amdgcn_sched_barrier(0);
#pragma unroll
        for (int p = 0; p < 14; ++p) {
          if (p == 8) { if (!last) BAR0(); STAMP(3); }
          va[p & 1] = SMF(mf[p >> 1], bu[p], p > 1 ? va[p & 1] : z4);
          __builtin_amdgcn_sched_barrier(0);
          if (p + 3 < 14) bu[p + 3] = BP(p + 3);
          if ((p & 1) == 0 && (p >> 1) + 2 < 7) mf[(p >> 1) + 2] = MF((p >> 1) + 2);
          __builtin_amdgcn_sched_barrier(0);
        }
#undef BP
      } else {
#pragma unroll
        for (int j = 0; j < 7; ++j) mf[j] = MF(j);
        bu[0] = BU(0);
        __builtin_amdgcn_sched_barrier(0);
#pragma unroll
        for (int j = 6; j >= 0; --j) va[0] = MFMA32(mf[j], mf[j], j < 6 ? va[0] : z4);
        asm volatile("" : "+v"(va[0]) :: "memory");
        __builtin_amdgcn_sched_barrier(0);
        bu[1] = BU(1); bu[2] = BU(2);
        const float n1 = ((const float*)(lds + L_N + 256))[8], n2 = ((const float*)(lds + L_N))[s];
        __builtin_amdgcn_sched_barrier(0);
#pragma unroll
        for (int j = 0; j < 7; ++j) {
          if (j == W3B) { if (!last) BAR0(); STAMP(3); }
          va[1] = SMF(mf[j], bu[j], j ? va[1] : z4);
          __builtin_amdgcn_sched_barrier(0);
          if (j + 3 < 7) { bu[j + 3] = BU(j + 3); mf[j + 3] = MF(j + 3); }
          __builtin_amdgcn_sched_barrier(0);
          if (j == 0 && !last) {
            float m = fmaxf(wave_max(absmax4(va[0])) * n1, n2);
            int e = ((int)(__float_as_uint(m) >> 23) - 125) >> 1;
            ((u32*)(lds + L_N + 256))[9] = (u32)(151 - e) << 23;
            __builtin_amdgcn_sched_barrier(0);
          }
        }
      }
#undef BU
      constexpr int T0 = ROLE == 0 ? 0 : 1;
      u2v vc[2]; u4v vz[2], vxf[2];
#pragma unroll
      for (int t = T0; t < 2; ++t) {
        vc[t] = __builtin_bit_cast(u2v, ck4(va[t]));
        vz[t] = (lg & 1) ? u4v{0u, 0u, vc[t][0], vc[t][1]} : u4v{vc[t][0], vc[t][1], 0u, 0u};
        if (!last) vxf[t] = *(const u4v*)(lds + L_V + (2 * wave + t - T0) * 1024 + lane * 16);
      }
      if (!last) {
        LAS char* pa = (LAS char*)lds + L_P + (l15 & 7) * 80; LAS char* pb = pa + 16 * lg; LAS char* ps = (LAS char*)lds + L_N + 256 + 36;
        const int hi = l15 >> 3;
        const unsigned wi = (jq * 7 + 2 * wave) * 64 + (lg * 8 + (l15 & 7)) * 2 + hi;
        unsigned ao = ((wi >> 3) * 16 + (wi & 7)) * 8, hm = (unsigned)-hi;
        u4v ones; u32 bias;
#pragma unroll
        for (int k = 0; k < 4; ++k) asm volatile("v_mov_b32 %0, 0x3f803f80" : "=v"(ones[k]));
        asm volatile("v_mov_b32 %0, 0x80000001" : "=v"(bias));
        asm volatile("" : "+v"(pa), "+v"(pb), "+v"(ps), "+v"(ao), "+v"(hm), "+v"(vz[1]));
        if (ROLE == 0) asm volatile("" : "+v"(vz[0]));
        char* gp = (char*)((a.sy + SY_ACC) + (long)s * 3584);
        asm volatile("" : "+s"(gp));
        __builtin_amdgcn_s_setprio(3);
        STAMP(7);
        BAR();
        STAMP(8);
        const u4v p0 = *(LAS const u4v*)(pa + 64), p12 = *(LAS const u4v*)pb;
        const u32 sb = *(LAS const u32*)ps;
        __builtin_amdgcn_sched_barrier(0);
        f4 den0 = MFMA32(ones, p0, z4), den1 = MFMA32(ones, p12, z4);
        f4 o[2];
#pragma unroll
        for (int t = T0; t < 2; ++t) o[t] = MFMA32(vz[t], p0, z4);
#pragma unroll
        for (int t = T0; t < 2; ++t) o[t] = MFMA32(vxf[t], p12, o[t]);
        __builtin_amdgcn_sched_barrier(0);
        const float r = __builtin_amdgcn_rcpf(__builtin_fmaf(den0[0], 0.25f, den1[0])) * __uint_as_float(sb);
#pragma unroll
        for (int t = T0; t < 2; ++t) {
          const u4v ou = __builtin_bit_cast(u4v, o[t]);
          f2 v = {__uint_as_float((ou[2] & hm) | (ou[0] & ~hm)), __uint_as_float((ou[3] & hm) | (ou[1] & ~hm))};
          v *= r;
          const u32 qh = (u32)(int)v[0] << 3, ql = ((u32)(int)v[1] << 6) + bias;
          __hip_atomic_fetch_add((GAS u64*)(gp + ao + (t - T0) * 1024), ((u64)qh << 32) | ql, RLX);
        }
        inv8 = __uint_as_float((251u << 23) - sb);
      }
      STAMP(4);
#pragma unroll
      for (int t = T0; t < 2; ++t)
        __hip_atomic_store((GAS u64*)(a.vm + ((long)(s * 64 + wg) * 7 + 2 * wave + t - T0) * 64 + lane), __builtin_bit_cast(u64, vc[t]), RLX);
    } else {
      const int lane = opq(threadIdx.x) & 63;
      u32 mgv = 0;
      if (!last) {
        asm volatile("s_waitcnt vmcnt(0)" ::: "memory");
        BAR();
        STAMP(3);
        f2 v[8];
#pragma unroll
        for (int w = 0; w < 8; ++w) v[w] = *(LAS const f2*)(ra + w * 1280);
        if (ROLE == 3) mgv = *(const u32*)(lds + L_H + hoff(wg >> 3, 56 * (wg & 7) + 2 * (lane < 28 ? lane : 0)));
        f2 sv = ((v[0] + v[1]) + (v[2] + v[3])) + ((v[4] + v[5]) + (v[6] + v[7]));
        float m0 = sv[0], m1 = sv[1];
        row_max2(m0, m1);
        const u32 pk = cvt2(__builtin_amdgcn_exp2f(sv[0] - m0), __builtin_amdgcn_exp2f(sv[1] - m1));
        asm volatile("ds_write_b16 %0, %1\n\tds_write_b16_d16_hi %0, %1 offset:80" :: "v"(wa), "v"(pk) : "memory");
        STAMP(7);
        BAR();
        STAMP(8);
        if (ROLE == 2) inv8 = __uint_as_float((251u << 23) - ((const u32*)(lds + L_N + 256))[9]);
      } else if (ROLE == 3) mgv = *(const u32*)(lds + L_H + hoff(wg >> 3, 56 * (wg & 7) + 2 * (lane < 28 ? lane : 0)));
      STAMP(4);
      if (ROLE == 3) {
        if (s > 0 && lane < 28) put_gran((a.sy + SY_MEMG) + (long)s * 1792 + wg * 28 + lane, s + 1, mgv);
        if (s > 0 && lane == 0) put_gran(a.sy + SY_FLV + (s - 1) * 64 + wg, s, 0);
      }
      if (DLY4) __builtin_amdgcn_s_sleep(DLY4);
      if (s + 1 < NSEG - 1) load_xgrp(a.x, s + 1, wave - 4, lds);
      if (ROLE == 3 && (s & 7) == 5 && s < 56) wait_vblk(a, (s >> 3) + 1);
    }
#undef MF
    STAMP(5);
    STAMP(6);
    STFLUSH();
  };
  __builtin_amdgcn_s_waitcnt(0x0F70);
  for (int s = 0; s < NSEG - 1; ++s) step(std::false_type{}, s);
  step(std::true_type{}, NSEG - 1);
  asm volatile("s_waitcnt vmcnt(0)" ::: "memory");
  BAR();
  if (ROLE == 3 && opq(threadIdx.x) == 448) put_gran(a.sy + SY_FLV + (NSEG - 1) * 64 + wg, NSEG, 0);
}

__device__ __forceinline__ void chain_body(const ChainArgs a, char* lds, int wg) {
  const int wave = __builtin_amdgcn_readfirstlane(opq(threadIdx.x) >> 6);
  if (wave < 3) chain_role<0>(a, lds, wg, wave);
  else if (wave == 3) chain_role<1>(a, lds, wg, 3);
  else if (wave < 7) chain_role<2>(a, lds, wg, wave);
  else chain_role<3>(a, lds, wg, 7);
}
#define PRI_QK 2
#define PRI_SM 0
#define PRI_PV 1
template <int N, int I = 0, class F> __device__ __forceinline__ void static_for(F&& f) {
  if constexpr (I < N) { f(std::integral_constant<int, I>{}); static_for<N, I + 1>(f); }
}
template <int OFF> __device__ __forceinline__ bf8 ds128(unsigned a) {
  bf8 r; asm volatile("ds_read_b128 %0, %1 offset:%2" : "=v"(r) : "v"(a), "n"(OFF) : "memory"); return r;
}
template <int OFF> __device__ __forceinline__ void dstr2(unsigned a, u2v& lo, u2v& hi) {
  asm volatile("ds_read_b64_tr_b16 %0, %2 offset:%3\n\tds_read_b64_tr_b16 %1, %2 offset:%4" : "=&v"(lo), "=&v"(hi) : "v"(a), "n"(OFF), "n"(OFF + 4096) : "memory");
}
template <int N, class T> __device__ __forceinline__ void lgkm(T& x, T& y) {
  if (&x == &y) asm volatile("s_waitcnt lgkmcnt(%1)" : "+v"(x) : "n"(N) : "memory");
  else asm volatile("s_waitcnt lgkmcnt(%2)" : "+v"(x), "+v"(y) : "n"(N) : "memory");
}
__device__ __forceinline__ void attn_item(char* lds, const u16* __restrict__ Q, const u16* __restrict__ K,
                                          const u16* __restrict__ V, u16* __restrict__ O, int h, int qb, u32* stp, u32* nq) {
  const int tid = opq(threadIdx.x), lane = tid & 63, wave = __builtin_amdgcn_readfirstlane(tid >> 6);
  const int q0 = qb * 128, rg = wave & 3, st = wave >> 2, qw = q0 + 32 * rg;
  const int hh = lane >> 5, l31 = lane & 31;
  const char* Kh = (const char*)(K + (long)h * S_ * DH); const char* Vh = (const char*)(V + (long)h * S_ * DH);
  bf8 qf[12];
  __syncthreads();
  const int nu = (q0 + 128) / 32;
#define GLDS(g, l, i) do { if (i == 0) __builtin_amdgcn_global_load_lds((const GAS void*)(g), (LAS void*)(l), 16, 0, 0); \
    else if (i == 1) __builtin_amdgcn_global_load_lds((const GAS void*)(g), (LAS void*)(l), 16, 1024, 0); \
    else if (i == 2) __builtin_amdgcn_global_load_lds((const GAS void*)(g), (LAS void*)(l), 16, 2048, 0); \
    else __builtin_amdgcn_global_load_lds((const GAS void*)(g), (LAS void*)(l), 16, 3072, 0); } while (0)
  auto issue = [&](int f) {
    const int ku = f + 3 < nu ? f + 3 : nu - 1, vu = f + 2 < nu ? f + 2 : nu - 1;
    char* kb = lds + ((f + 3) & 3) * 16384 + rg * 4096; char* vb = lds + 65536 + ((f + 2) & 3) * 16384 + rg * 4096;
    const char* kg = Kh + (long)(ku * 32 + 8 * rg) * DH * 2; const char* vg = Vh + (long)(vu * 32 + 8 * rg) * DH * 2;
    const unsigned ln = (unsigned)opq(threadIdx.x) & 63u, h2 = ln >> 5, pos = ln & 31u;
    const unsigned ko = h2 * DH * 2 + ((pos ^ (8 * (rg & 1) + h2)) << 4), vo = h2 * DH * 2 + ((pos ^ (h2 << 2)) << 4);
#pragma unroll
    for (int i = 0; i < 4; ++i)
      GLDS(kg + (ko ^ (unsigned)(i << 5)), kb, i);
    if (f > -3) {
#pragma unroll
      for (int i = 0; i < 4; ++i)
        GLDS(vg + (vo ^ (unsigned)((i & 1) << 7)), vb, i);
    }
  };
#define PHASE_QK() asm volatile("s_waitcnt vmcnt(8)\n\ts_barrier" ::: "memory")
#define PHASE_PV(f) do { asm volatile("s_barrier" ::: "memory"); issue(f); } while (0)
  f16v o[8];
#pragma unroll
  for (int i = 0; i < 8; ++i)
#pragma unroll
    for (int j = 0; j < 16; ++j) o[i][j] = 0.f;
  float m = 0.f, lsum = 0.f;
  bool first = true;
  if (st) issue(-2); else { issue(-3); issue(-1); }
  {
    const u16* qp = Q + (long)(h * S_ + qw + l31) * DH + 8 * hh;
#pragma unroll
    for (int ks = 0; ks < 16; ++ks) {
      bf8 t = *(const bf8*)(qp + 16 * ks);
      if (ks < 12) qf[ks] = t; else *(bf8*)(lds + 131072 + wave * 4096 + (ks - 12) * 1024 + lane * 16) = t;
    }
  }
  asm volatile("s_waitcnt lgkmcnt(0)" ::: "memory");
#define AST(k)
  if (st) PHASE_PV(0);
  for (int u = st; u < nu; u += 2) {
    const int k0 = u * 32;
    const bool live = k0 <= qw + 31;
    bf8 pf[2]; float alpha = 1.f;
    f16v s;
#pragma unroll
    for (int j = 0; j < 16; ++j) s[j] = -m;
    asm volatile("" : "+v"(s));
    AST(5);
    PHASE_QK();
    AST(0);
    if (live) {
      __builtin_amdgcn_s_setprio(PRI_QK);
      const unsigned ln = (unsigned)opq(threadIdx.x) & 63u;
      const unsigned ka = (unsigned)(size_t)(LAS char*)lds + (u & 3) * 16384 + (ln & 31u) * 512 + (((ln >> 5) ^ (ln & 15u)) << 4);
      const unsigned qa = (unsigned)(size_t)(LAS char*)lds + 131072 + wave * 4096 + ln * 16;
      bf8 kf[16], ql[4];
#define KF(ks) kf[ks] = ds128<((ks) >> 3) * 256>(ka ^ (unsigned)(((ks) & 7) << 5))
      static_for<4>([&](auto KS) { KF(KS.value); });
      static_for<16>([&](auto KS) {
        constexpr int ks = KS.value, n = ks <= 8 ? 3 : ks <= 11 ? ks - 5 : 30 - 2 * ks;
        if constexpr (ks < 12) lgkm<n>(kf[ks], kf[ks]); else lgkm<n>(kf[ks], ql[ks - 12]);
        if constexpr (ks < 12) s = __builtin_amdgcn_mfma_f32_32x32x16_bf16(kf[ks], qf[ks], s, 0, 0, 0);
        else s = __builtin_amdgcn_mfma_f32_32x32x16_bf16(kf[ks], ql[ks - 12], s, 0, 0, 0);
        __builtin_amdgcn_sched_barrier(0);
        if constexpr (ks + 4 < 16) KF(ks + 4);
        if constexpr (ks >= 8 && ks < 12) ql[ks - 8] = ds128<(ks - 8) * 1024>(qa);
        __builtin_amdgcn_sched_barrier(0);
      });
#undef KF
      __builtin_amdgcn_s_setprio(PRI_SM);
      AST(1);
      float mx = -INFINITY;
      if (k0 + 31 > qw) {
#pragma unroll
        for (int j = 0; j < 16; ++j) { int key = k0 + (j & 3) + 8 * (j >> 2) + 4 * hh; if (key > qw + l31) s[j] = -INFINITY; }
      }
#pragma unroll
      for (int j = 0; j < 16; ++j) mx = fmaxf(mx, s[j]);
      mx = swap32_max(mx);
      if (first || __builtin_amdgcn_ballot_w64(mx > 8.f) != 0) {
        const float d = first ? mx : fmaxf(mx, 0.f);
        alpha = first ? 1.f : __builtin_amdgcn_exp2f(-d);
        m += d; first = false;
#pragma unroll
        for (int j = 0; j < 16; ++j) s[j] -= d;
      }
      float ps = 0.f;
#pragma unroll
      for (int j = 0; j < 16; ++j) { float p = __builtin_amdgcn_exp2f(s[j]); s[j] = p; ps += p; }
      lsum = lsum * alpha + ps;
#pragma unroll
      for (int ss = 0; ss < 2; ++ss) {
        u4v t = {cvt2(s[8 * ss + 0], s[8 * ss + 1]), cvt2(s[8 * ss + 2], s[8 * ss + 3]),
                 cvt2(s[8 * ss + 4], s[8 * ss + 5]), cvt2(s[8 * ss + 6], s[8 * ss + 7])};
        pf[ss] = as_bf8(t);
      }
      __builtin_amdgcn_s_setprio(PRI_PV);
    }
    AST(2);
    PHASE_PV(u + 1);
    AST(3);
    if (live) {
      if (__builtin_amdgcn_ballot_w64(alpha != 1.f) != 0) {
#pragma unroll
        for (int i = 0; i < 8; ++i)
#pragma unroll
          for (int j = 0; j < 16; ++j) o[i][j] *= alpha;
      }
      AST(4);
      const unsigned l2 = (unsigned)opq(threadIdx.x) & 63u, blk = (l2 >> 4) & 1, q4 = (l2 & 15) >> 2, p4 = l2 & 3;
      const unsigned va = (unsigned)(size_t)(LAS char*)lds + 65536 + (u & 3) * 16384 + (4 * (l2 >> 5) + q4) * 512 + (((2 * blk + (p4 >> 1)) ^ (q4 << 2)) << 4) + 8 * (p4 & 1);
      u2v vl[16], vh[16];
#define VF(i) dstr2<((i) >> 3) * 256 + ((i) & 1) * 8192>(va ^ (unsigned)((((i) >> 1) & 3) << 6), vl[i], vh[i])
      static_for<4>([&](auto I) { VF(I.value); });
      static_for<16>([&](auto I) {
        constexpr int i = I.value;
        lgkm<(i <= 12 ? 6 : 30 - 2 * i)>(vl[i], vh[i]);
        o[i >> 1] = __builtin_amdgcn_mfma_f32_32x32x16_bf16(as_bf8(u4v{vl[i][0], vl[i][1], vh[i][0], vh[i][1]}), pf[i & 1], o[i >> 1], 0, 0, 0);
        __builtin_amdgcn_sched_barrier(0);
        if constexpr (i + 4 < 16) VF(i + 4);
        __builtin_amdgcn_sched_barrier(0);
      });
#undef VF
    }
  }
  if (!st) PHASE_QK();
  __builtin_amdgcn_s_setprio(0);
#undef PHASE_QK
#undef PHASE_PV
  asm volatile("s_waitcnt vmcnt(0)" ::: "memory");
  const u32 tk = tk_draw(nq);
  lsum = swap32_add(lsum);
  if (first) m = -INFINITY;
  const int ln = opq(threadIdx.x) & 63;
  float* ml = (float*)(lds + 131072 + wave * 4096);
  ml[ln] = m; ml[64 + ln] = lsum;
  LBAR();
  {
    const float* mo = (const float*)(lds + 131072 + (wave ^ 4) * 4096);
    const float m2 = mo[ln], l2 = mo[64 + ln], mn = fmaxf(m, m2);
    const float a1 = __builtin_amdgcn_exp2f(m - mn), a2 = __builtin_amdgcn_exp2f(m2 - mn);
    const float f = a1 / (lsum * a1 + l2 * a2);
#pragma unroll
    for (int i = 0; i < 8; ++i)
#pragma unroll
      for (int j = 0; j < 16; ++j) o[i][j] *= f;
  }
  {
    char* xb = lds + wave * 16384 + ln * 16;
#pragma unroll
    for (int i = 0; i < 4; ++i)
#pragma unroll
      for (int g = 0; g < 4; ++g) {
        f4 v = st ? f4{o[i][4 * g], o[i][4 * g + 1], o[i][4 * g + 2], o[i][4 * g + 3]} : f4{o[4 + i][4 * g], o[4 + i][4 * g + 1], o[4 + i][4 * g + 2], o[4 + i][4 * g + 3]};
        *(f4*)(xb + (i * 4 + g) * 1024) = v;
      }
  }
  LBAR();
  u2v r[16];
  {
    const char* xb = lds + (wave ^ 4) * 16384 + ln * 16;
#pragma unroll
    for (int i = 0; i < 4; ++i)
#pragma unroll
      for (int g = 0; g < 4; ++g) {
        f4 v = *(const f4*)(xb + (i * 4 + g) * 1024);
        f4 w = st ? f4{o[4 + i][4 * g], o[4 + i][4 * g + 1], o[4 + i][4 * g + 2], o[4 + i][4 * g + 3]} : f4{o[i][4 * g], o[i][4 * g + 1], o[i][4 * g + 2], o[i][4 * g + 3]};
        v += w;
        r[i * 4 + g] = u2v{cvt2(v[0], v[1]), cvt2(v[2], v[3])};
      }
  }
  char* ob = lds + (wave ^ 4) * 16384;
  {
    const int hh = ln >> 5, l31 = ln & 31;
    asm volatile("s_waitcnt lgkmcnt(0)" ::: "memory");
#pragma unroll
    for (int i = 0; i < 4; ++i)
#pragma unroll
      for (int g = 0; g < 4; ++g) *(u2v*)(ob + l31 * 272 + (32 * i + 8 * g + 4 * hh) * 2) = r[i * 4 + g];
    asm volatile("s_waitcnt lgkmcnt(0)" ::: "memory");
    u16* op = O + (long)(h * S_ + qw) * DH + 128 * st;
#pragma unroll
    for (int it = 0; it < 8; ++it) {
      int rr = 4 * it + (ln >> 4);
      st16_sc1(op + rr * DH + (ln & 15) * 8, *(const u4v*)(ob + rr * 272 + (ln & 15) * 16));
    }
  }
  tk_post(lds, tk);
}
#define TSLP 8
constexpr int L_QW = 0;
__device__ __forceinline__ void wait_cnt(u64* sy, int ci, u32 need) {
  if (threadIdx.x < 64) {
    u32* c = cnt_ptr(sy, ci); u32* err = (u32*)(sy + SY_ERR);
    for (int spins = 0;; ++spins) {
      if (peek(c) >= need) break;
      if (spins > SPIN_CAP) { __hip_atomic_store((GAS u32*)err, 1u, RLX); break; }
      if ((spins & 63) == 63 && peek(err)) break;
      __builtin_amdgcn_s_sleep(TSLP);
    }
    __builtin_amdgcn_fence(__ATOMIC_ACQUIRE, "agent");
    asm volatile("s_waitcnt vmcnt(0)" ::: "memory");
  }
  __syncthreads();
}
__device__ __forceinline__ void publish_cnt(u64* sy, int ci) {
  asm volatile("s_waitcnt vmcnt(0)" ::: "memory");
  __syncthreads();
  if (threadIdx.x == 0) __hip_atomic_fetch_add((GAS u32*)cnt_ptr(sy, ci), 1u, RLX);
}
__device__ __forceinline__ int dequeue(u64* sy, int ci, char* lds) {
  __syncthreads();
  if (threadIdx.x == 0) *(volatile int*)(lds + L_QW) = (int)__hip_atomic_fetch_add((GAS u32*)cnt_ptr(sy, ci), 1u, RLX);
  __syncthreads();
  return __builtin_amdgcn_readfirstlane(*(volatile int*)(lds + L_QW));
}
__device__ __forceinline__ void wait_gran_slow(u64* sy, u64* g, u32 tag, bool on) {
  u32* err = (u32*)(sy + SY_ERR);
  for (int spins = 0;; ++spins) {
    u64 x = (u64)tag << 32;
    if (on) x = __hip_atomic_load((GAS u64*)g, RLX);
    if (__builtin_amdgcn_ballot_w64((u32)(x >> 32) != tag) == 0) break;
    if (spins > SPIN_CAP) { __hip_atomic_store((GAS u32*)err, 1u, RLX); break; }
    if ((spins & 63) == 63 && peek(err)) break;
    __builtin_amdgcn_s_sleep(TSLP);
  }
}

struct TailArgs { const char* TU; float* PP; u16* out; };
constexpr int L_TP = 1024, L_TV = L_TP + 49152;
__device__ __forceinline__ u2v bld8(rsrc_t r, unsigned v, unsigned s) { return __builtin_amdgcn_raw_buffer_load_b64(r, v, s, 16); }
template <bool FULL>
__device__ __forceinline__ void tail_item(const ChainArgs a, const TailArgs t, char* lds, int s, int u0, int jq, u32* nq) {
  const int tid = opq(threadIdx.x), lane = tid & 63, wave = __builtin_amdgcn_readfirstlane(tid >> 6);
  const int l15 = lane & 15, lg = lane >> 4;
  constexpr int NT = FULL ? 4 : 1, NH2 = FULL ? 4 : 16;
  const f4 z4 = {0.f, 0.f, 0.f, 0.f};
  u32* err = (u32*)(a.sy + SY_ERR);
  u64* fv = a.sy + SY_FLV + s * 64 + (FULL ? lane : l15 * 4 + jq);
  if (wave == 7) load_xseg(a.x, s, lds, lane);
  if (wave == 0) {
    const u32* c = cnt_ptr(a.sy, (lane & 1 ? C_T : C_V) + (s >> 3));
    bool all = true; int one = 0;
    for (int spins = 0;; ++spins) {
      u32 v = 224; u64 x = (u64)(s + 1) << 32;
      if (lane < 2) v = __hip_atomic_load((GAS u32*)c, RLX);
      if (FULL && (all || lane == one)) x = __hip_atomic_load((GAS u64*)fv, RLX);
      const u64 bad = __builtin_amdgcn_ballot_w64((u32)(x >> 32) != (u32)(s + 1));
      if (__builtin_amdgcn_ballot_w64(v < 224) == 0 && bad == 0) { if (all) break; all = true; continue; }
      if (bad) { all = false; one = __builtin_ctzll(bad); }
      if (spins > SPIN_CAP) { __hip_atomic_store((GAS u32*)err, 1u, RLX); break; }
      if ((spins & 63) == 63 && peek(err)) break;
      __builtin_amdgcn_s_sleep(TSLP);
    }
    asm volatile("" ::: "memory");
  }
  asm volatile("s_barrier" ::: "memory");
  SB();
  const rsrc_t rt = mk_rsrc(t.TU + (long)(2 * s * 224 + 28 * wave) * 1024);
  const int ct0 = wave < 7 ? (FULL ? 4 * wave : 7 * jq + wave) : 0;
  const rsrc_t rv = mk_rsrc(a.VU + (long)(s * 448 + ct0) * 1024), rm = mk_rsrc((const char*)a.vm + (long)(s * 448 + ct0) * 512);
  const unsigned vo = lane * 16;
  u4v tf[2][14], vf[NH2][NT]; u2v vm[NH2][NT];
  auto get_mem = [&]() {
    if (tid < 448) {
      if (s == 0) { int r = tid / 56, c8 = tid % 56; *(u4v*)(lds + L_H + hoff(r, c8 * 8)) = *(const u4v*)(a.mem0 + r * DM + c8 * 8); }
      else {
        u64* gm = (a.sy + SY_MEMG) + (long)s * 1792 + tid;
        u64* p[4] = {gm, gm + 448, gm + 896, gm + 1344}; u32 v[4];
        get_gran<4>(p, s + 1, v, err);
        int hi = tid >= 224 ? 1 : 0, cp = tid - 224 * hi;
#pragma unroll
        for (int k = 0; k < 4; ++k) *(u32*)(lds + L_H + hoff(2 * k + hi, 2 * cp)) = v[k];
      }
    }
  };
  if (FULL) get_mem();
  SB();
#pragma unroll
  for (int ks = 0; ks < 14; ++ks)
#pragma unroll
    for (int e = 0; e < 2; ++e) tf[e][ks] = bld(rt, vo + (ks & 3) * 1024, (unsigned)((FULL ? 0 : u0) * 224 + e * 14 + (ks & ~3)) * 1024u);
  if (!FULL) {
#pragma unroll
    for (int h = 0; h < 9; ++h) vf[h][0] = bld(rv, vo, h * 28672);
    if (wave < 7) {
      const char* vp = a.VU + (long)(s * 448 + ct0) * 1024 + vo;
#pragma unroll
      for (int h = 9; h < 16; ++h)
        __builtin_amdgcn_global_load_lds((const GAS void*)(vp + h * 28672), (LAS void*)(lds + L_TV + wave * 7168 + (h - 9) * 1024), 16, 0, 16);
    }
  }
  SB();
  if (wave == 7) { if (FULL) asm volatile("s_waitcnt vmcnt(28)" ::: "memory"); else asm volatile("s_waitcnt vmcnt(37)" ::: "memory"); }
  asm volatile("s_waitcnt lgkmcnt(0)\n\ts_barrier" ::: "memory");
  SB();
#define HF(kt, c) cat8(*(const s4*)(lds + L_H + hoff(KB3[kt] + l15, (c) + 4 * lg)), *(const s4*)(lds + L_H + hoff(KB3[kt] + l15, (c) + 16 + 4 * lg)))
#pragma unroll
  for (int ur = 0; ur < (FULL ? 2 : 1); ++ur) {
    const int u = FULL ? ur : u0;
    f4 sv[2][3];
#pragma unroll
    for (int ks = 0; ks < 14; ++ks) {
#pragma unroll
      for (int kt = FULL ? 0 : 1; kt < 3; ++kt) {
        if (FULL && ur == 0 && kt == 2) { sv[0][2] = z4; sv[1][2] = z4; continue; }
        const s8 hf = HF(kt, 32 * ks);
#pragma unroll
        for (int e = 0; e < 2; ++e) sv[e][kt] = MFMA32(hf, tf[e][ks], ks ? sv[e][kt] : z4);
      }
      if (FULL) {
        SB();
        if (ur == 0) {
#pragma unroll
          for (int e = 0; e < 2; ++e) tf[e][ks] = bld(rt, vo + (ks & 3) * 1024, (unsigned)(224 + e * 14 + (ks & ~3)) * 1024u);
        } else if (ks >= 2 && ks < 10) {
          const int h = (ks - 2) >> 1;
#pragma unroll
          for (int k = 2 * (ks & 1); k < 2 * (ks & 1) + 2; ++k) { vf[h][k % NT] = bld(rv, vo + k * 1024, h * 28672); vm[h][k % NT] = bld8(rm, lane * 8 + k * 512, h * 14336); }
        }
        SB();
      }
    }
    if (!FULL) {
      if (wave == 0) wait_gran_slow(a.sy, (a.sy + SY_MEMG) + (long)s * 1792 + (2 * jq + u0) * 64, s + 1, lane == 0);
      asm volatile("s_barrier" ::: "memory");
      SB();
      u64 x = (u64)(s + 1) << 32;
      if (lane < 16) x = __hip_atomic_load((GAS u64*)fv, RLX);
      get_mem();
      if (__builtin_amdgcn_ballot_w64((u32)(x >> 32) != (u32)(s + 1))) wait_gran_slow(a.sy, fv, s + 1, lane < 16);
      asm volatile("" ::: "memory");
      SB();
#pragma unroll
      for (int h = 0; h < 16; ++h) vm[h][0] = bld8(rm, lane * 8, h * 14336);
      SB();
      asm volatile("s_waitcnt lgkmcnt(0)\n\ts_barrier" ::: "memory");
      SB();
#pragma unroll
      for (int ks = 0; ks < 14; ++ks) {
        const s8 hf = HF(0, 32 * ks);
#pragma unroll
        for (int e = 0; e < 2; ++e) sv[e][0] = MFMA32(hf, tf[e][ks], ks ? sv[e][0] : z4);
      }
    }
    const int tq = 16 * u + l15;
#pragma unroll
    for (int e = 0; e < 2; ++e) {
      float mx = -INFINITY;
#pragma unroll
      for (int kt = 0; kt < 3; ++kt)
#pragma unroll
        for (int r = 0; r < 4; ++r) {
          bool ok = kt == 0 ? lg < 2 : (16 * (kt - 1) + 4 * lg + r <= tq);
          sv[e][kt][r] = ok ? sv[e][kt][r] : -INFINITY; mx = fmaxf(mx, sv[e][kt][r]);
        }
      mx = swap32_max(swap16_max(mx));
      float sum = 0.f;
#pragma unroll
      for (int kt = 0; kt < 3; ++kt)
#pragma unroll
        for (int r = 0; r < 4; ++r) { float p = __builtin_amdgcn_exp2f(sv[e][kt][r] - mx); sv[e][kt][r] = p; sum += p; }
      sum = swap32_add(swap16_add(sum));
      const float inv = 1.f / sum;
      char* pp = lds + L_TP + ((2 * wave + e) * 2 + ur) * 1536;
      *(u4v*)(pp + lane * 16) = cvt8(sv[e][1] * inv, sv[e][2] * inv);
      *(u2v*)(pp + 1024 + lane * 8) = __builtin_bit_cast(u2v, ck4(sv[e][0] * inv));
    }
  }
#undef HF
  asm volatile("s_waitcnt lgkmcnt(0)\n\ts_barrier" ::: "memory");
  SB();
  if (wave < 7) {
    f4 o[FULL ? 2 : 1][NT], o2[FULL ? 2 : 1][NT];
#pragma unroll
    for (int h = 0; h < 16; ++h) {
#pragma unroll
      for (int ur = 0; ur < (FULL ? 2 : 1); ++ur) {
        const char* pp = lds + L_TP + (h * 2 + ur) * 1536;
        const u4v p12 = *(const u4v*)(pp + lane * 16); const u2v p0 = *(const u2v*)(pp + 1024 + (lane & 31) * 8);
        if (!FULL && h >= 9) vf[h][0] = *(const u4v*)(lds + L_TV + wave * 7168 + (h - 9) * 1024 + vo);
#pragma unroll
        for (int k = 0; k < NT; ++k) o[ur][k] = MFMA32(vf[h % NH2][k], p12, h ? o[ur][k] : z4);
#pragma unroll
        for (int k = 0; k < NT; ++k) o2[ur][k] = MFMA16(__builtin_bit_cast(s4, vm[h % NH2][k]), __builtin_bit_cast(s4, p0), h ? o2[ur][k] : z4);
      }
      if (FULL && h + 4 < 16) {
        SB();
#pragma unroll
        for (int k = 0; k < NT; ++k) { vf[h % NH2][k] = bld(rv, vo + k * 1024, (h + 4) * 28672); vm[h % NH2][k] = bld8(rm, lane * 8 + k * 512, (h + 4) * 14336); }
        SB();
      }
    }
#pragma unroll
    for (int ur = 0; ur < (FULL ? 2 : 1); ++ur)
#pragma unroll
      for (int k = 0; k < NT; ++k)
        *(s4*)(t.out + (long)(32 * s + 16 * (FULL ? ur : u0) + l15) * 896 + 16 * (ct0 + k) + 4 * lg) = ck4(o[ur][k] + o2[ur][k]);
  } else tk_post(lds, tk_draw(nq, 448), 448);
  LBAR();
}
struct MegaArgs {
  ChainArgs c; TailArgs t;
  const u16* Bx; const u16* ckv; const u16* wq; const u16* wuk; const u16* wuv; const u16* wo;
  u16* Qb; u16* Kb; u16* Vb; u16* Ctx; char* TU; char* VU; char* XU;
  const u16* Wq; const u16* Wk; const u16* Wv; const u16* Wo;
};
__device__ __forceinline__ u4v unit_fix(u4v a) {
  auto p = __builtin_amdgcn_permlane16_swap(a[0], a[2], false, false);
  auto q = __builtin_amdgcn_permlane16_swap(a[1], a[3], false, false);
  return u4v{p[0], p[1], q[0], q[1]};
}
__device__ __forceinline__ u64 tag_init(u64 e, char* lds) {
  e = ((u64)(u32)__builtin_amdgcn_readfirstlane((int)(e >> 32)) << 32) | (u32)__builtin_amdgcn_readfirstlane((int)e);
  const u64 tag = e * 6364136223846793005ull + 1442695040888963407ull;
  if ((opq(threadIdx.x) & 63) == 0) { *(volatile LAS u64*)(lds + L_PC) = tag; *(volatile LAS u64*)(lds + L_PC + 8) = 0; }
  return tag;
}
__device__ __forceinline__ void arrive(char* lds, int ci, u32 n, u64* flag) {
  if ((opq(threadIdx.x) & 63) == 0) {
    const u32 o = __hip_atomic_fetch_add((LAS u32*)(lds + L_PC + 8 + 4 * ci), 1u, __ATOMIC_RELAXED, __HIP_MEMORY_SCOPE_WORKGROUP);
    if (o == n - 1) __hip_atomic_store((GAS u64*)flag, *(volatile LAS u64*)(lds + L_PC), RLX);
  }
}
__device__ __forceinline__ u4v bld0(rsrc_t r, unsigned v, unsigned s) { return __builtin_amdgcn_raw_buffer_load_b128(r, v, s, 0); }
#define QST(k)
#define XLD bld
template <bool BS>
__device__ __forceinline__ void pro_task(const MegaArgs& g, char* lds, int h, int rbk, int vb) {
  const int tid = opq(threadIdx.x), lane = tid & 63, wave = __builtin_amdgcn_readfirstlane(tid >> 6), l15 = lane & 15, lg = lane >> 4;
  const int u = lane >> 3, c = lane & 7;
  const f4 z4 = {0.f, 0.f, 0.f, 0.f};
  u64 e = 0;
  if (tid == 448) e = __hip_atomic_load((GAS u64*)g.c.pf, RLX);
  SB();
  u4v rw[4];
  if (!BS) {
    const char* src = (const char*)(g.Wk + (long)(64 * rbk + 8 * wave + u) * HD + h * DH) + c * 16;
#pragma unroll
    for (int i = 0; i < 4; ++i) rw[i] = *(const u4v*)(src + i * 128);
  } else {
#pragma unroll
    for (int i = 0; i < 4; ++i) {
      const int ee = 32 * wave + 8 * i + u, gg = ((ee >> 1) & 1) | (((ee >> 4) & 1) << 1);
      rw[i] = *(const u4v*)((const char*)(g.Wo + (long)(h * DH + ee) * DM + 64 * rbk) + ((c ^ (2 * gg)) << 4));
    }
  }
  SB();
  asm volatile("s_barrier" ::: "memory");
  SB();
  const unsigned fw = (c & 1) * 1024 + (c >> 1) * 256 + ((u ^ c) << 4);
  char* rwp = BS ? lds + wave * 4096 + lane * 16 : lds + (wave >> 1) * 8192 + (wave & 1) * 128 + fw;
  const char* ru = BS ? g.c.BsU : g.c.AtU;
  const unsigned ub = (unsigned)((h * 28 + 4 * rbk) * 14 + 2 * wave) * 1024u;
  char* us = lds + 32768;
  if (wave < 7) {
    u4v cl[4][8];
    {
      const rsrc_t rc = mk_rsrc((BS ? g.Wv : g.Wq) + (long)64 * wave * HD + h * DH);
      const unsigned vo = (8 * ((u >> 1) & 1) + 4 * (u >> 2) + (u & 1)) * 8192 + c * 16;
#pragma unroll
      for (int q = 0; q < 4; ++q)
#pragma unroll
        for (int i = 0; i < 8; ++i) cl[q][i] = bld0(rc, vo + ((q + rbk) & 3) * 128, (32 * (i >> 2) + 2 * ((i >> 1) & 1) + 16 * (i & 1)) * 8192);
    }
    SB();
    QST(12);
#pragma unroll
    for (int i = 0; i < 4; ++i) *(u4v*)(rwp + i * (BS ? 1024 : 2048)) = rw[i];
    asm volatile("s_waitcnt lgkmcnt(0)\n\ts_barrier" ::: "memory");
    QST(13);
    f4 acc[4][4];
    const unsigned q4 = l15 >> 2, p = lane & 3, gg = (q4 >> 1) | ((lg & 1) << 1);
    const unsigned tb = (16 * lg + q4) * 128 + (p >> 1) * 16 + 8 * (p & 1);
    const unsigned fr = lg * 256 + ((l15 ^ (2 * lg)) << 4);
    char* st = us + wave * 8192;
#pragma unroll
    for (int q = 0; q < 4; ++q) {
#pragma unroll
      for (int i = 0; i < 8; ++i) *(u4v*)(st + (i >> 1) * 2048 + (i & 1) * 128 + fw) = cl[q][i];
#pragma unroll
      for (int tt = 0; tt < 2; ++tt) {
        const int t = 2 * q + tt, qr = (q + rbk) & 3;
        bf8 af[4], bf[4];
#pragma unroll
        for (int a = 0; a < 4; ++a) af[a] = as_bf8(*(const u4v*)(st + a * 2048 + tt * 1024 + (fr ^ (tt << 4))));
#pragma unroll
        for (int r = 0; r < 4; ++r) {
          if (!BS) bf[r] = *(const bf8*)(lds + (r * 8 + 2 * qr + tt) * 1024 + (fr ^ (tt << 4)));
          else {
            const unsigned o = tb + ((r ^ gg) << 5) + (64 * qr + 8 * tt) * 128;
            bf[r] = __builtin_bit_cast(bf8, cat8(tr_read(lds, o), tr_read(lds, o + 512)));
          }
        }
#pragma unroll
        for (int r = 0; r < 4; ++r)
#pragma unroll
          for (int a = 0; a < 4; ++a) acc[r][a] = MFMA32(af[a], bf[r], t ? acc[r][a] : z4);
      }
    }
    QST(14);
    constexpr float cs = BS ? 1.f : 0.0625f * 1.4426950408889634f;
    float m = 0.f;
#pragma unroll
    for (int r = 0; r < 4; ++r) {
      u4v uo[2];
#pragma unroll
      for (int k = 0; k < 2; ++k) {
        uo[k] = BS ? cvt8(acc[r][2 * k], acc[r][2 * k + 1]) : cvt8(acc[r][2 * k] * cs, acc[r][2 * k + 1] * cs);
        bst(uo[k], ru, lane * 16 + k * 1024, ub + r * 14336);
        if (BS) *(u4v*)(st + (r * 2 + k) * 1024 + lane * 16) = uo[k];
      }
      if (BS) m = fmaxf(m, absmax4(MFMA32(uo[1], uo[1], MFMA32(uo[0], uo[0], z4))));
    }
    if (BS) {
      m = wave_max(m);
      if (lane == 0) __hip_atomic_store((GAS float*)(g.c.pf + PN1) + (h * 7 + rbk) * 8 + wave, m, RLX);
    }
  } else {
    const u64 tag = tag_init(e, lds);
#pragma unroll
    for (int i = 0; i < 4; ++i) *(u4v*)(rwp + i * (BS ? 1024 : 2048)) = rw[i];
    asm volatile("s_waitcnt lgkmcnt(0)\n\ts_barrier" ::: "memory");
    wait_flags(g.c.pf + (lane < 8 ? PF1 + 28 + (lane >> 2) * 32 + (lane & 3) : PFX), tag, BS ? lane < 9 : lane == 8 && vb >= 64);
  }
  asm volatile("s_waitcnt vmcnt(0) lgkmcnt(0)\n\ts_barrier" ::: "memory");
  if (tid == 448) __hip_atomic_store((GAS u64*)(g.c.pf + PF1 + vb), *(volatile LAS u64*)(lds + L_PC), RLX);
  if (!BS) return;
  SB();
  QST(7);
  const rsrc_t rx = mk_rsrc(g.XU); const char* rv = g.VU;
  const unsigned xo = (unsigned)((wave >> 1) * 56 + 2 * (wave & 1)) * 1024u;
  u4v xf[2][14];
#pragma unroll
  for (int ks = 0; ks < 14; ++ks) { xf[0][ks] = XLD(rx, lane * 16, xo + ks * 4096); xf[1][ks] = XLD(rx, lane * 16 + 1024, xo + ks * 4096); }
  SB();
  QST(15);
  SB();
#pragma unroll
  for (int ps = 0; ps < 2; ++ps) {
    f4 acc[4][2];
#pragma unroll
    for (int ks = 0; ks < 14; ++ks) {
      bf8 w[4];
#pragma unroll
      for (int r = 0; r < 4; ++r) w[r] = *(const bf8*)(us + (ks >> 1) * 8192 + (r * 2 + (ks & 1)) * 1024 + lane * 16);
#pragma unroll
      for (int r = 0; r < 4; ++r) { acc[r][0] = MFMA32(xf[0][ks], w[r], ks ? acc[r][0] : z4); acc[r][1] = MFMA32(xf[1][ks], w[r], ks ? acc[r][1] : z4); }
      if (ps == 0) { xf[0][ks] = XLD(rx, lane * 16, xo + 4 * 57344 + ks * 4096); xf[1][ks] = XLD(rx, lane * 16 + 1024, xo + 4 * 57344 + ks * 4096); }
      if (ks == 5 && ps == 1) {
        SB();
        asm volatile("s_waitcnt vmcnt(0)" ::: "memory"); arrive(lds, 1, 8, g.c.pf + PF2 + h * 8 + rbk);
        SB();
      }
    }
#pragma unroll
    for (int r = 0; r < 4; ++r) bst(cvt8(acc[r][0], acc[r][1]), rv, lane * 16 + r * 1024, (unsigned)((8 * ps + wave) * 448 + h * 28 + 4 * rbk) * 1024u);
    SB();
  }
  asm volatile("s_waitcnt vmcnt(0)" ::: "memory");
}
__device__ __forceinline__ void pro_misc(const MegaArgs& g, char* lds, int m, int vb) {
  const int tid = opq(threadIdx.x), lane = tid & 63, wave = __builtin_amdgcn_readfirstlane(tid >> 6), l15 = lane & 15, lg = lane >> 4;
  u64 e = 0;
  if (tid == 448) e = __hip_atomic_load((GAS u64*)g.c.pf, RLX);
  SB();
  if (m == 8 && wave == 7) {
#pragma unroll
    for (int k = 0; k < 3; ++k) __hip_atomic_store((GAS u32*)cnt_ptr(g.c.sy, 64 * k + lane), 0u, RLX);
    __hip_atomic_store((GAS u32*)(g.c.sy + SY_ERR), 0u, RLX);
    asm volatile("s_waitcnt vmcnt(0)" ::: "memory");
    const u64 tag = tag_init(e, lds);
    if (lane == 0) __hip_atomic_store((GAS u64*)(g.c.pf + PFX), tag, RLX);
  }
  SB();
  u4v xf[7];
  const char* xp = (const char*)(g.c.x + (long)(64 * m + 16 * (wave & 3) + l15) * DM + 8 * lg) + (wave >> 2) * 448;
#pragma unroll
  for (int k = 0; k < 7; ++k) xf[k] = *(const u4v*)(xp + k * 64);
  SB();
  if (m >= 8) {
    constexpr int NZ = SY_CNT / 2;
    for (int i = (m - 8) * 512 + tid; i < NZ; i += 24 * 512) bst(u4v{0u, 0u, 0u, 0u}, g.c.sy, (unsigned)i * 16u, 0);
  }
  SB();
  QST(12);
#pragma unroll
  for (int k = 0; k < 7; ++k) bst(unit_fix(xf[k]), g.XU, lane * 16, (unsigned)(m * 56 + 4 * (7 * (wave >> 2) + k) + (wave & 3)) * 1024u);
  SB();
  f4 gm = {0.f, 0.f, 0.f, 0.f};
#pragma unroll
  for (int k = 0; k < 7; ++k) gm = MFMA32(xf[k], xf[k], gm);
  const float mx = wave_max(absmax4(gm));
  if (lane == 0) __hip_atomic_store((GAS float*)(g.c.pf + PN2) + m * 8 + wave, mx, RLX);
  if (wave == 7) wait_flags(g.c.pf + PFX, tag_init(e, lds), lane == 0 && m > 8);
  QST(13);
  asm volatile("s_waitcnt vmcnt(0) lgkmcnt(0)\n\ts_barrier" ::: "memory");
  if (tid == 448) __hip_atomic_store((GAS u64*)(g.c.pf + PF1 + vb), *(volatile LAS u64*)(lds + L_PC), RLX);
  QST(14);
}
template <bool P3>
__device__ __forceinline__ void s5_item(char* lds, const MegaArgs& g, int nb, int tg0, int nit, u32* nq) {
  const int tid = opq(threadIdx.x), lane = tid & 63, wave = __builtin_amdgcn_readfirstlane(tid >> 6);
  const f4 z4 = {0.f, 0.f, 0.f, 0.f};
  __syncthreads();
  {
    const char* src = (P3 ? g.c.AtU : g.c.BsU) + (long)(nb * 8 + wave) * 14336 + lane * 16;
#pragma unroll
    for (int i = 0; i < 14; ++i)
      __builtin_amdgcn_global_load_lds((const GAS void*)(src + i * 1024), (LAS void*)(lds + wave * 14336 + i * 1024), 16, 0, 16);
  }
  const rsrc_t rx = mk_rsrc(g.XU); const char* ro = P3 ? g.TU : g.VU;
  const unsigned vo = lane * 16;
  int tg = tg0 + wave;
  unsigned xc = (unsigned)tg * 57344u, xn = nit > 1 ? xc + 8 * 57344u : xc;
  u4v xr[14]; bf8 wf[8]; f4 acc[8][4];
#pragma unroll
  for (int f = 0; f < 14; ++f) xr[f] = bld(rx, vo + (f & 3) * 1024, xc + (f >> 2) * 4096);
  SB();
  asm volatile("s_waitcnt vmcnt(14)\n\ts_barrier" ::: "memory");
  LAS const char* w0 = (LAS const char*)lds + vo; LAS const char* w1 = w0 + 4 * 14336;
  asm volatile("" : "+v"(w0), "+v"(w1));
#define WF(rt, ks) (*(LAS const bf8*)(((rt) < 4 ? w0 : w1) + ((rt) & 3) * 14336 + (ks) * 1024))
#pragma unroll
  for (int rt = 0; rt < 8; ++rt) wf[rt] = WF(rt, 0);
  SB();
  auto step = [&](auto KS) {
    constexpr int ks = KS.value;
#pragma unroll
    for (int rt = 0; rt < 8; ++rt) {
#pragma unroll
      for (int t = 0; t < 4; ++t) {
        if (P3) acc[rt][t] = MFMA32(wf[rt], xr[(4 * ks + t) % 14], ks ? acc[rt][t] : z4);
        else acc[rt][t] = MFMA32(xr[(4 * ks + t) % 14], wf[rt], ks ? acc[rt][t] : z4);
      }
      SB();
      wf[rt] = WF(rt, (ks + 1) % 14);
      SB();
    }
#pragma unroll
    for (int t = 0; t < 4; ++t) {
      constexpr int f0 = 4 * ks + 14;
      const int f = f0 + t;
      xr[f % 14] = f < 56 ? bld(rx, vo + (f & 3) * 1024, xc + (f >> 2) * 4096) : bld(rx, vo + (f & 3) * 1024, xn + ((f - 56) >> 2) * 4096);
    }
    SB();
  };
  u32* cp = cnt_ptr(g.c.sy, P3 ? C_T : C_V); int inc = 0;
  for (int it = 1;; ++it) {
    static_for<4>(step);
    {
      int t0, t1;
      asm volatile("s_waitcnt vmcnt(16)\n\ts_mov_b64 exec, 1\n\tv_mov_b32 %0, %2\n\tv_mov_b32 %1, 0\n\tglobal_atomic_add %1, %0, %3\n\ts_mov_b64 exec, -1"
                   : "=&v"(t0), "=&v"(t1) : "s"(inc), "s"(cp) : "memory");
      SB();
    }
    static_for<14, 4>(step);
    SB();
    if (P3) {
#pragma unroll
      for (int t = 0; t < 4; ++t)
#pragma unroll
        for (int p = 0; p < 4; ++p) bst(cvt8(acc[2 * p][t], acc[2 * p + 1][t]), ro, vo + p * 1024, (unsigned)((4 * tg + t) * 224 + 4 * nb) * 1024u);
    } else {
#pragma unroll
      for (int rt = 0; rt < 8; ++rt)
#pragma unroll
        for (int G = 0; G < 2; ++G) bst(cvt8(acc[rt][2 * G], acc[rt][2 * G + 1]), ro, vo + (rt & 3) * 1024, (unsigned)((2 * tg + G) * 448 + 8 * nb + (rt & 4)) * 1024u);
    }
    SB();
    cp = cnt_ptr(g.c.sy, (P3 ? C_T : C_V) + (tg >> 2)); inc = 1;
    if (it >= nit) break;
    tg += 8; xc = xn; xn = it + 1 < nit ? xc + 8 * 57344u : xc;
  }
  const u32 tk = tk_draw(nq);
  asm volatile("s_waitcnt vmcnt(0)" ::: "memory");
  if (lane == 0) __hip_atomic_fetch_add((GAS u32*)cp, 1u, RLX);
  tk_post(lds, tk);
  LBAR();
#undef WF
}
constexpr int I_P1 = 14, I_ATT = 78, L_GQ = 131072;
__device__ __forceinline__ int list_c(int x) { return x % 3 == 1 ? 18 : 7; }
__device__ __forceinline__ int list_len(int x) { return I_ATT + 32 + list_c(x); }
__device__ __forceinline__ int list_0(int x) { return x < 2 ? I_P1 : 0; }
struct GUnit { const char* R; const char* C; char* O; int nt, ldc, cnt, kind; };
typedef const __attribute__((address_space(4))) MegaArgs* KArgs;
__device__ __forceinline__ GUnit g8_decode(int x, int i) {
  KArgs gp = (KArgs)__builtin_amdgcn_kernarg_segment_ptr();
  asm volatile("" : "+s"(gp));
#define g (*gp)
  GUnit u;
  const int y = x % 3, hc = 12 + 2 * (x / 3), nkc = y < 2 ? 11 : 10;
  int j = i - I_P1, h, mat, mb;
  if (j < 16) { h = x; mat = j & 1; mb = j >> 1; }
  else if ((j -= 16) < nkc) { j += 11 * y; h = hc + (j >> 4); mat = j & 1; mb = (j >> 1) & 7; }
  else if ((j -= nkc) < 8) { h = x; mat = 2; mb = j; }
  else if ((j -= 8) < 16) { h = x + 6; mat = j & 1; mb = j >> 1; }
  else if ((j -= 16) < 16 - nkc) { j += 5 * y; h = hc + (j >> 3); mat = 2; mb = j & 7; }
  else { h = x + 6; mat = 2; mb = j - (16 - nkc); }
  u.R = (const char*)((mat == 0 ? g.wuk : mat == 1 ? g.wuv : g.wq) + h * 256);
  u.ldc = mat == 2 ? 896 : 2048;
  u.C = (const char*)(mat == 2 ? g.Bx : g.ckv) + (long)mb * 256 * u.ldc;
  u.O = (char*)((mat == 0 ? g.Kb : mat == 1 ? g.Vb : g.Qb) + (long)(h * S_ + mb * 256) * DH);
  u.nt = mat == 2 ? 7 : 16; u.cnt = C_H + h; u.kind = mat == 2 ? 3 : 2;
#undef g
  return u;
}
__device__ __forceinline__ void glds2(unsigned l, unsigned voff, const char* g0, const char* g1) {
  asm volatile("s_mov_b32 m0, %0\n\ts_nop 0\n\tglobal_load_lds_dwordx4 %1, %2\n\ts_add_u32 m0, %0, 0x400\n\ts_nop 0\n\tglobal_load_lds_dwordx4 %1, %3"
               :: "s"(l), "v"(voff), "s"(g0), "s"(g1) : "memory", "m0", "scc");
}
template <int OFF> __device__ __forceinline__ void st16s(unsigned voff, char* base, u4v v) {
  asm volatile("global_store_dwordx4 %0, %1, %2 offset:%3 sc1\n\ts_nop 1" :: "v"(voff), "v"(v), "s"(base), "n"(OFF) : "memory");
}
#define G8_BAR() do { __builtin_amdgcn_sched_barrier(0); asm volatile("s_barrier" ::: "memory"); __builtin_amdgcn_sched_barrier(0); } while (0)
#define G8_WV(n) asm volatile("s_waitcnt vmcnt(" #n ")" ::: "memory")
#define G8_WL(n) asm volatile("s_waitcnt lgkmcnt(" #n ")" ::: "memory")
__device__ __forceinline__ int g8_run(char* lds, const MegaArgs& g, int x, int i0, u32* stp) {
  const int tid = opq(threadIdx.x), lane = tid & 63, wid = __builtin_amdgcn_readfirstlane(tid >> 6);
  const int wr = wid >> 2, wc = wid & 3, fr = lane & 15, fq = lane >> 4;
  const unsigned lb = (unsigned)(size_t)(LAS char*)lds, lw = lb + wid * 2048;
  GUnit cur = g8_decode(x, i0), nxt = cur;
  int inext = 0; bool has_next = false;
  unsigned aC, aR[4];
  {
    const unsigned sw = (unsigned)((fr * 64 + fq * 16) ^ ((fr >> 3) << 5)), q = fr >> 2, p = lane & 3;
    aC = lb + 65536 + wc * 4096 + sw;
#pragma unroll
    for (int m = 0; m < 4; ++m) aR[m] = lb + 256 * (8 * fq + q) + 16 * ((8 * wr + 2 * m + (p >> 1)) ^ ((q << 2) | ((fq & 1) << 1))) + 8 * (p & 1);
  }
  const char* sR; const char* sC; unsigned vR, vC; int hC;
  constexpr int kR = 64 * 8192, hR = 256, iR = 32768, oL = 512;
  auto cursor = [&](const GUnit& u) {
    const unsigned ln = (unsigned)opq(threadIdx.x) & 63u;
    const unsigned row = 16 * wid + (ln >> 2), ch = ((ln & 3) ^ ((ln >> 5) << 1)) << 4;
    vC = row * (unsigned)u.ldc + ch;
    vR = (8 * wid + (ln >> 4)) * 8192u + (((ln & 15) ^ (((ln >> 4) << 2) | ((wid & 1) << 1))) << 4);
    sR = u.R; sC = u.C; hC = 128 * u.ldc;
  };
#define ST_R(b, h) glds2(lw + (b) + (h) * 16384, vR, sR + (h) * hR, sR + (h) * hR + iR)
#define ST_C(b, h) glds2(lw + 65536 + (b) + (h) * 16384, vC, sC + (h) * hC, sC + (h) * hC + 64)
  f4 acc[2][2][4][2];
#pragma unroll
  for (int a = 0; a < 16; ++a) { acc[a >> 3][(a >> 2) & 1][a & 3][0] = f4{0.f, 0.f, 0.f, 0.f}; acc[a >> 3][(a >> 2) & 1][a & 3][1] = f4{0.f, 0.f, 0.f, 0.f}; }
  bf8 Rf[4][2], C0[2][2], C1[2][2];
#define LD_C(dst, h) do { _Pragma("unroll") for (int n = 0; n < 2; ++n) _Pragma("unroll") for (int k = 0; k < 2; ++k) \
    dst[n][k] = *(LAS const bf8*)(size_t)(aC + (h) * 16384 + n * 2048 + k * 1024); } while (0)
#define LD_R(h) do { _Pragma("unroll") for (int m = 0; m < 4; ++m) _Pragma("unroll") for (int k = 0; k < 2; ++k) { \
      s4 lo = __builtin_amdgcn_ds_read_tr16_b64_v4i16((LAS s4*)(size_t)(aR[m] + (h) * 16384 + k * 8192)); \
      s4 hi = __builtin_amdgcn_ds_read_tr16_b64_v4i16((LAS s4*)(size_t)(aR[m] + (h) * 16384 + k * 8192 + 1024)); \
      Rf[m][k] = __builtin_bit_cast(bf8, cat8(lo, hi)); } } while (0)
#define MMA(ai, bj, Cf) do { __builtin_amdgcn_s_setprio(1); _Pragma("unroll") for (int k = 0; k < 2; ++k) _Pragma("unroll") for (int m = 0; m < 4; ++m) _Pragma("unroll") for (int n = 0; n < 2; ++n) \
    acc[ai][bj][m][n] = __builtin_amdgcn_mfma_f32_16x16x32_bf16(Rf[m][k], Cf[n][k], acc[ai][bj][m][n], 0, 0, 0); __builtin_amdgcn_s_setprio(0); } while (0)
  char* oB = cur.O; int oC = cur.cnt, oK = cur.kind; unsigned vO = 0;
  auto epi = [&](auto AI, auto BJ) {
    constexpr int ai = AI.value, bj = BJ.value;
#pragma unroll
    for (int n = 0; n < 2; ++n) {
      if (oK == 3) {
#pragma unroll
        for (int m = 0; m < 4; ++m) acc[ai][bj][m][n] *= 0.0625f * 1.4426950408889634f;
      }
      u4v X, Y;
#pragma unroll
      for (int d = 0; d < 4; ++d) {
        X[d] = cvt2(acc[ai][bj][d >> 1][n][2 * (d & 1)], acc[ai][bj][d >> 1][n][2 * (d & 1) + 1]);
        Y[d] = cvt2(acc[ai][bj][2 + (d >> 1)][n][2 * (d & 1)], acc[ai][bj][2 + (d >> 1)][n][2 * (d & 1) + 1]);
      }
      char* ob = oB + (long)(128 * bj + 16 * n) * oL;
      u4v S1, S2;
#pragma unroll
      for (int d = 0; d < 4; ++d) {
        S1[d] = (u32)__builtin_amdgcn_update_dpp((int)X[d], (int)Y[d], 0x128, 0xf, 0xc, false);
        S2[d] = (u32)__builtin_amdgcn_update_dpp((int)Y[d], (int)X[d], 0x128, 0xf, 0x3, false);
      }
      st16s<256 * ai>(vO, ob, S1); st16s<256 * ai>(vO, ob + 8 * oL, S2);
#pragma unroll
      for (int m = 0; m < 4; ++m) acc[ai][bj][m][n] = f4{0.f, 0.f, 0.f, 0.f};
    }
  };
  using I0 = std::integral_constant<int, 0>; using I1 = std::integral_constant<int, 1>;
#define G8_T()
  __syncthreads();
  cursor(cur);
  ST_C(0, 0); ST_R(0, 0); ST_C(0, 1); ST_R(0, 1);
  if (wr == 1) G8_BAR();
  G8_WV(4); G8_BAR();
  sR += kR; sC += 128;
  ST_C(32768, 0); ST_R(32768, 0); ST_C(32768, 1);
  G8_WV(6); G8_BAR();
  int t = 0; unsigned b = 0; bool prev = false; int dq = 0;
  for (;;) {
    const int r = cur.nt - 1 - t;
    const bool fin = t == 0 && prev;
    LD_C(C0, 0); __builtin_amdgcn_sched_barrier(0); LD_R(0); __builtin_amdgcn_sched_barrier(0);
    ST_R(b ^ 32768, 1);
    if (r == 1) cursor(nxt); else { sR += kR; sC += 128; }
    if (fin) epi(I1{}, I1{});
    G8_WL(15);
    G8_BAR(); G8_WL(0); MMA(0, 0, C0); G8_BAR();
    LD_C(C1, 1); __builtin_amdgcn_sched_barrier(0);
    ST_C(b, 0);
    if (r == 0) {
      const int ln = opq(threadIdx.x) & 63;
      oB = cur.O; oC = cur.cnt; oK = cur.kind;
      vO = (unsigned)((32 * wc + (ln & 7)) * oL + 128 * wr + 64 * ((ln >> 3) & 1) + 16 * (ln >> 4));
      epi(I0{}, I0{});
    }
    if (r == 2) {
      inext = __builtin_amdgcn_readfirstlane(*(volatile LAS int*)(size_t)(lb + L_GQ)) + list_0(x) + 32;
      has_next = inext < I_ATT;
      if (has_next) nxt = g8_decode(x, inext);
    }
    G8_BAR(); G8_WL(0); MMA(0, 1, C1); G8_BAR();
    LD_R(1); __builtin_amdgcn_sched_barrier(0);
    ST_R(b, 0);
    if (r == 0) epi(I0{}, I1{});
    G8_BAR(); G8_WL(0); MMA(1, 0, C0); G8_BAR();
    ST_C(b, 1);
    if (r == 0) { epi(I1{}, I0{}); G8_WV(18); } else if (fin) G8_WV(10); else G8_WV(6);
    if (t == 1 && prev && lane == 0) __hip_atomic_fetch_add((GAS u32*)cnt_ptr(g.c.sy, oC), 1u, RLX);
    {
      int tmp;
      asm volatile("s_cmp_eq_u32 %2, 3\n\ts_cbranch_scc1 L3_%=\n\ts_cmp_lg_u32 %2, 4\n\ts_cbranch_scc1 L2_%=\n\t"
                   "s_mov_b64 exec, 1\n\tv_mov_b32 %0, 1\n\tv_mov_b32 %1, 0\n\tglobal_atomic_add %0, %1, %0, %3 sc0\n\ts_mov_b64 exec, -1\n\ts_branch L2_%=\n"
                   "L3_%=:\n\ts_mov_b64 exec, 1\n\tv_mov_b32 %1, %4\n\tds_write_b32 %1, %0\n\ts_mov_b64 exec, -1\n"
                   "L2_%=:"
                   : "+v"(dq), "=&v"(tmp) : "s"(wid == 0 ? r : 0), "s"(cnt_ptr(g.c.sy, C_X + x)), "s"(lb + L_GQ) : "memory", "scc");
    }
    G8_BAR(); MMA(1, 1, C1); G8_BAR();
    b ^= 32768; aC ^= 32768;
#pragma unroll
    for (int m = 0; m < 4; ++m) aR[m] ^= 32768;
    if (r == 0) { if (!has_next) break; G8_T(); cur = nxt; t = 0; prev = true; } else ++t;
  }
  epi(I1{}, I1{});
  G8_WV(0);
  if (lane == 0) __hip_atomic_fetch_add((GAS u32*)cnt_ptr(g.c.sy, oC), 1u, RLX);
  if (wr == 0) G8_BAR();
  G8_BAR();
#undef ST_R
#undef ST_C
#undef LD_C
#undef LD_R
#undef MMA
  return inext;
}
constexpr int MEGA_LDS = 163840;
#define PST(k)
#define IT0()
#define ITA(k)
__global__ __launch_bounds__(512, 2) void mega_kernel(MegaArgs g) {
  extern __shared__ __attribute__((aligned(16))) char lds[];
  const int vb = (blockIdx.x & 7) * 32 + (blockIdx.x >> 3);
  const int wave = __builtin_amdgcn_readfirstlane(threadIdx.x >> 6);
  u64* sy = g.c.sy;
  PST(0);
  {
    const int xc = vb >> 5, ix = vb & 31;
    if (ix >= 28) pro_misc(g, lds, xc * 4 + ix - 28, vb);
    else if (xc < 4) pro_task<false>(g, lds, 4 * xc + ix / 7, ix % 7, vb);
    else pro_task<true>(g, lds, 4 * (xc - 4) + ix / 7, ix % 7, vb);
    PST(10);
    if (vb < 64) {
      chain_body(g.c, lds, vb);
      PST(1);
      return;
    }
    if (xc >= 4 && ix < 28) {
      asm volatile("s_barrier" ::: "memory");
      if (threadIdx.x < 2) __hip_atomic_fetch_add((GAS u32*)cnt_ptr(sy, C_V + threadIdx.x), 2u, RLX);
    }
  }
  const int xl = (vb >> 5) - 2;
  u32* lq = cnt_ptr(sy, C_X + xl);
  int i = list_0(xl) + (vb & 31);
  auto wait_go = [&]() { if (wave == 7) grid_wait(g.c.pf, *(volatile LAS u64*)(lds + L_PC)); };
  IT0();
  if (i < I_P1) {
    wait_go();
    PST(11);
    s5_item<false>(lds, g, 14 * (xl - 2) + i, 8, 3, lq);
    i = tk_read(lds) + 32;
    ITA(1);
  }
  if (i < I_ATT) {
    i = g8_run(lds, g, xl, i, (u32*)((char*)(sy + SY_ERR) + 4096) + vb * 32);
    PST(1);
  }
  if ((vb & 31) + list_0(xl) >= I_P1) wait_go();
  const int n3 = xl < 2 ? 56 : 0;
  for (; i < I_ATT + n3; i = tk_read(lds) + list_0(xl) + 32) {
    const int k = i - I_ATT;
    s5_item<true>(lds, g, 28 * xl + (k < 28 ? k : k - 28), k < 28 ? 0 : 16, 2, lq);
    ITA(6);
  }
  for (; i < list_len(xl) + n3; i = tk_read(lds) + list_0(xl) + 32) {
    {
      const int j = i - I_ATT - n3, y = xl % 3, hc = 12 + 2 * (xl / 3);
      int h, qb;
      if (y != 1) {
        if (j < 12) { h = j & 1 ? hc + (y >> 1) : xl; qb = 15 - (j >> 1); }
        else if (j < 18) { h = xl + 6; qb = 27 - j; }
        else if (j == 18) { h = hc + (y >> 1); qb = 9; }
        else { h = xl + 6 * ((j - 19) & 1); qb = 9 - ((j - 19) >> 1); }
      } else {
        if (j < 8) { h = xl; qb = 15 - j; }
        else if (j < 12) { h = hc + (j & 1); qb = 8 - ((j - 8) >> 1); }
        else if (j < 20) { h = xl + 6; qb = 27 - j; }
        else if (j < 22) { h = xl + 6 * (j & 1); qb = 7; }
        else { const int k = j - 22; h = k & 2 ? hc + (k & 1) : xl + 6 * (k & 1); qb = 6 - (k >> 2); }
      }
      wait_cnt(sy, C_H + h, 192);
      ITA(4);
      attn_item(lds, g.Qb, g.Kb, g.Vb, g.Ctx, h, qb, (u32*)((char*)(sy + SY_ERR) + 4096), lq);
      publish_cnt(sy, C_C + qb * 4 + (h >> 2));
      ITA(5);
      PST(2);
    }
  }
  PST(3);
  PST(4);
  u32* gq = cnt_ptr(sy, C_Q0 + 4);
  int tk = dequeue(sy, C_Q0 + 4, lds);
  ITA(0);
  for (; tk < 248; tk = tk_read(lds)) {
    int p = -1, s = 0;
    if (tk < 40) s = tk; else if (tk < 88) p = tk - 40; else if (tk < 96) s = tk - 48; else if (tk < 144) p = tk - 48;
    else if (tk < 148) s = tk - 96; else if (tk < 180) p = tk - 52; else s = tk - 128;
    if (p >= 0) {
      const int rb = (int)(0xfedcba9012384765ull >> (4 * (p >> 3))) & 15, kq = (0x9c >> (2 * ((p >> 1) & 3))) & 3, n0 = (p & 1) * 224;
      wait_cnt(sy, C_C + rb * 4 + kq, 4);
      ITA(7);
      p5_item(lds, g.Ctx + (long)(kq * 4 * S_ + rb * 128) * DH, g.wo + (long)kq * 1024 * DM + n0, g.t.out + (long)rb * 128 * 896 + 448 + n0,
              (char*)g.t.PP + (long)(rb * 2 + (p & 1)) * 458752, kq, cnt_ptr(sy, C_P + rb * 2 + (p & 1)), gq);
      ITA(8);
      PST(5);
    } else if (s < 56) { tail_item<true>(g.c, g.t, lds, s, 0, 0, gq); ITA(9); PST(4); }
    else { s -= 56; tail_item<false>(g.c, g.t, lds, 56 + (s >> 3), s & 1, (s >> 1) & 3, gq); ITA(10); }
  }
  PST(6);
}

extern "C" void kernel_launch(void* const* d_in, const int* in_sizes, int n_in, void* d_out, int out_size, void* d_ws, size_t ws_size,
                              hipStream_t stream) {
  char* ws = (char*)d_ws;
  size_t off = 0;
  auto alloc = [&](size_t bytes) { char* p = ws + off; off += (bytes + 4095) & ~(size_t)4095; return p; };
  MegaArgs g;
  g.c.sy = (u64*)alloc((size_t)SY_END * 8);
  g.c.pf = (u64*)alloc((size_t)PF_END * 8);
  const size_t big = (size_t)S_ * HD * 2, big2 = (size_t)S_ * NH * DM * 2;
  g.c.AtU = alloc((size_t)NH * DM * DM * 2); g.c.BsU = alloc((size_t)NH * DM * DM * 2);
  g.c.x = (const u16*)d_in[0]; g.c.mem0 = (const u16*)d_in[1]; g.c.vm = (u64*)alloc((size_t)64 * 64 * 7 * 64 * 8);
  g.TU = alloc(big2); g.VU = alloc(big2); g.XU = alloc((size_t)S_ * DM * 2);
  g.t.TU = g.TU; g.c.VU = g.VU; g.t.PP = (float*)alloc((size_t)256 * 65536); g.t.out = (u16*)d_out;
  g.Wq = (const u16*)d_in[2]; g.Wk = (const u16*)d_in[3]; g.Wv = (const u16*)d_in[4]; g.Wo = (const u16*)d_in[5];
  g.Bx = (const u16*)d_in[7]; g.ckv = (const u16*)d_in[8]; g.wq = (const u16*)d_in[9]; g.wuk = (const u16*)d_in[10]; g.wuv = (const u16*)d_in[11]; g.wo = (const u16*)d_in[12];
  g.Qb = (u16*)alloc(big); g.Kb = (u16*)alloc(big); g.Vb = (u16*)alloc(big); g.Ctx = (u16*)alloc(big);
  (void)hipFuncSetAttribute((const void*)mega_kernel, hipFuncAttributeMaxDynamicSharedMemorySize, MEGA_LDS);
  mega_kernel<<<256, 512, MEGA_LDS, stream>>>(g);
}
```
